```python
import torch

def _softmax(x, axis=-1):
    return torch.softmax(x, dim=axis)

def _gelu(x):
    return torch.nn.functional.gelu(x, approximate='tanh')

def _silu(x):
    return torch.nn.functional.silu(x)

def _sigmoid(x):
    return torch.sigmoid(x)

def _layernorm(x, axis=-1, eps=1e-05):
    m = x.mean(dim=axis, keepdim=True)
    v = x.var(dim=axis, keepdim=True, unbiased=False)
    return (x - m) / torch.sqrt(v + eps)

def _scatter_add(tgt, idx, src, axis=0):
    return tgt.clone().index_add_(axis, idx, src)

def _segment_sum(src, seg, n):
    out = torch.zeros((n,) + src.shape[1:], dtype=src.dtype)
    return out.index_add_(0, seg, src)

def _pad(x, pw):
    flat = tuple((v for pair in reversed(pw) for v in pair))
    return torch.nn.functional.pad(x, flat)
kernel_name = 'gated_mix__ssm_conv_s4__mixer_gmlp__prompt'

def setup_inputs():

    _w = lambda a: (lambda t: t.to(torch.bfloat16) if t.is_floating_point() else t)(torch.as_tensor(a))

    def _A_setup():
        B, L, D, N = (1, 2048, 640, 192)
        A_re = -(lambda _x: torch.exp(torch.as_tensor(_x)))((lambda *s: torch.randn(*s, dtype=torch.bfloat16) * 3.2)(D, N))
        A_im = torch.pi * (lambda *s: torch.randn(*s, dtype=torch.bfloat16) * 0.1)(D, N)
        B_re = (lambda *s: torch.randn(*s, dtype=torch.bfloat16) * 0.8)(D, N)
        B_im = (lambda *s: torch.randn(*s, dtype=torch.bfloat16) * 0.8)(D, N)
        C_re = (lambda *s: torch.randn(*s, dtype=torch.bfloat16) * 0.8)(D, N)
        C_im = (lambda *s: torch.randn(*s, dtype=torch.bfloat16) * 0.8)(D, N)
        log_dt = (lambda *s: torch.randn(*s, dtype=torch.bfloat16) * 0.1)(D) * 0.5 - 4.0
        Dskip = (lambda *s: torch.randn(*s, dtype=torch.bfloat16) * 0.8)(D)
        return {'u': _w((lambda *s: torch.randn(*s, dtype=torch.bfloat16) * 0.05)(B, L, D)), 'A_re': _w(A_re), 'A_im': _w(A_im), 'B_re': _w(B_re), 'B_im': _w(B_im), 'C_re': _w(C_re), 'C_im': _w(C_im), 'log_dt': _w(log_dt), 'Dskip': _w(Dskip)}
    _A_in = _A_setup()

    def _B_setup():
        B, S, D, F = (1, 2048, 640, 1536)
        Fh = F // 2
        return {'x': _w((lambda *s: torch.randn(*s, dtype=torch.bfloat16) * 0.05)(B, S, D)), 'w_in': _w((lambda *s: torch.randn(*s, dtype=torch.bfloat16) * 0.4)(D, 2 * Fh)), 'w_sp': _w(0.01 * (lambda *s: torch.randn(*s, dtype=torch.bfloat16) * 0.4)(S, S)), 'b_sp': _w(1.0 + 0.01 * (lambda *s: torch.randn(*s, dtype=torch.bfloat16) * 0.05)(S)), 'w_out': _w((lambda *s: torch.randn(*s, dtype=torch.bfloat16) * 0.05)(Fh, D))}
    _B_in = _B_setup()
    return {'A_u': _A_in['u'], 'A_A_re': _A_in['A_re'], 'A_A_im': _A_in['A_im'], 'A_B_re': _A_in['B_re'], 'A_B_im': _A_in['B_im'], 'A_C_re': _A_in['C_re'], 'A_C_im': _A_in['C_im'], 'A_log_dt': _A_in['log_dt'], 'A_Dskip': _A_in['Dskip'], 'B_x': _B_in['x'], 'B_w_in': _B_in['w_in'], 'B_w_sp': _B_in['w_sp'], 'B_b_sp': _B_in['b_sp'], 'B_w_out': _B_in['w_out'], 'gate_w': _w((lambda *s: torch.randn(*s, dtype=torch.bfloat16) * 0.05)(640) * 16.0)}

def reference(A_u, A_A_re, A_A_im, A_B_re, A_B_im, A_C_re, A_C_im, A_log_dt, A_Dskip, B_x, B_w_in, B_w_sp, B_b_sp, B_w_out, gate_w):

    def _A_ref(u, A_re, A_im, B_re, B_im, C_re, C_im, log_dt, Dskip):
        Bsz, L, D = u.shape
        dt = torch.exp(log_dt)[:, None]
        A = A_re + 1j * A_im
        Bc = B_re + 1j * B_im
        Cc = C_re + 1j * C_im
        logdA = dt * A
        dB = dt * Bc
        l = (lambda x, dt: x.to(dt))(torch.arange(L), A_re.dtype)
        P = torch.exp(logdA[:, :, None] * l[None, None, :])
        K = torch.einsum('dn,dnl,dn->dl', Cc, P, dB).real
        L2 = 2 * L
        Kp = _pad(K, ((0, 0), (0, L)))
        up = (lambda x, *d: x.permute(*d))(u, 0, 2, 1)
        up = _pad(up, ((0, 0), (0, 0), (0, L)))
        Kf = (lambda x, axes=(-2, -1): torch.fft.rfft2(x.float(), dim=axes))(Kp[:, None, :], axes=(-2, -1))
        Uf = (lambda x, axes=(-2, -1): torch.fft.rfft2(x.float(), dim=axes))(up[:, :, None, :], axes=(-2, -1))
        Yf = Uf * Kf[None, :, :, :]
        y = (lambda x, s, axes=(-2, -1): torch.fft.irfft2(x, s=s, dim=axes).to(torch.bfloat16))(Yf, (1, L2), axes=(-2, -1))[:, :, 0, :L]
        y = (lambda x, *d: x.permute(*d))(y, 0, 2, 1) + u * Dskip[None, None, :]
        return y

    def _B_ref(x, w_in, w_sp, b_sp, w_out):
        B, S, D = x.shape
        F = w_in.shape[-1]
        Fh = F // 2
        z = _gelu(torch.einsum('bsd,df->bsf', x, w_in))
        z1 = z[..., :Fh].clone()
        z2 = _layernorm(z[..., Fh:], axis=-1)
        g = torch.einsum('ts,bsf->btf', w_sp, z2) + b_sp[None, :, None]
        return torch.einsum('bsf,fd->bsd', z1 * g, w_out)
    x = A_u
    g = _sigmoid((x * gate_w).sum(-1, keepdims=True))
    a = _A_ref(x, A_A_re, A_A_im, A_B_re, A_B_im, A_C_re, A_C_im, A_log_dt, A_Dskip)
    b = _B_ref(x, B_w_in, B_w_sp, B_b_sp, B_w_out)
    return g * a + (1 - g) * b
```

```cpp
#include <hip/hip_runtime.h>
#include <hip/hip_bf16.h>
typedef unsigned short u16;
__device__ __forceinline__ float bf2f(u16 v){ return __uint_as_float(((unsigned)v)<<16); }
__device__ __forceinline__ u16 f2bf(float f){ unsigned u=__float_as_uint(f); return (u16)((u + 0x7FFF + ((u>>16)&1))>>16); }
constexpr int L=2048, D=640, N=192, F=1536, FH=768;

__global__ void k_kgen(const u16* Are,const u16* Aim,const u16* Bre,const u16* Bim,const u16* Cre,const u16* Cim,const u16* logdt,float* K){
  int d=blockIdx.x;
  float dt=bf2f(f2bf(expf(bf2f(logdt[d]))));
  for(int l=threadIdx.x;l<L;l+=blockDim.x){
    float lf=bf2f(f2bf((float)l));
    float acc=0;
    for(int n=0;n<N;n++){
      float ar=dt*bf2f(Are[d*N+n]), ai=dt*bf2f(Aim[d*N+n]);
      float br=dt*bf2f(Bre[d*N+n]), bi=dt*bf2f(Bim[d*N+n]);
      float cr=bf2f(Cre[d*N+n]), ci=bf2f(Cim[d*N+n]);
      float wr=cr*br-ci*bi, wi=cr*bi+ci*br;
      float e=expf(ar*lf); float s,c; sincosf(ai*lf,&s,&c);
      acc+=e*(wr*c-wi*s);
    }
    K[d*L+l]=acc;
  }
}
__global__ void k_conv(const u16* u,const float* K,float* y){
  int d=blockIdx.x;
  __shared__ float su[L], sk[L];
  for(int i=threadIdx.x;i<L;i+=blockDim.x){ su[i]=bf2f(u[i*D+d]); sk[i]=K[d*L+i]; }
  __syncthreads();
  for(int t=threadIdx.x;t<L;t+=blockDim.x){
    float acc=0; for(int l=0;l<=t;l++) acc+=sk[l]*su[t-l];
    y[t*D+d]=acc;
  }
}
__global__ void k_gemm1(const u16* x,const u16* w,float* z){
  int s=blockIdx.x; __shared__ float sx[D]; __shared__ float red[2][256];
  for(int i=threadIdx.x;i<D;i+=256) sx[i]=bf2f(x[s*D+i]);
  __syncthreads();
  float v[6]; float sm=0,sq=0;
  for(int j=0;j<6;j++){ int f=threadIdx.x+256*j; float a=0; for(int k=0;k<D;k++) a+=sx[k]*bf2f(w[k*F+f]);
    float g=0.5f*a*(1.f+tanhf(0.7978845608f*(a+0.044715f*a*a*a))); v[j]=g; if(j>=3){sm+=g;} }
  red[0][threadIdx.x]=sm; __syncthreads();
  float m=0; for(int i=0;i<256;i++) m+=red[0][i]; m/=FH;
  for(int j=3;j<6;j++) sq+=(v[j]-m)*(v[j]-m);
  red[1][threadIdx.x]=sq; __syncthreads();
  float vv=0; for(int i=0;i<256;i++) vv+=red[1][i]; vv/=FH;
  float r=rsqrtf(vv+1e-5f);
  for(int j=0;j<6;j++){ int f=threadIdx.x+256*j; z[s*F+f]= j<3? v[j] : (v[j]-m)*r; }
}
__global__ void k_gemm2(const u16* wsp,const u16* bsp,const float* z,float* h){
  int t=blockIdx.x; __shared__ float sw[L];
  for(int i=threadIdx.x;i<L;i+=256) sw[i]=bf2f(wsp[t*L+i]);
  __syncthreads();
  for(int j=0;j<3;j++){ int f=threadIdx.x+256*j; float a=0; for(int s=0;s<L;s++) a+=sw[s]*z[s*F+FH+f];
    h[t*FH+f]=(a+bf2f(bsp[t]))*z[t*F+f]; }
}
__global__ void k_gemm3(const float* h,const u16* wo,const u16* u,const u16* dskip,const u16* gw,const float* y,u16* out){
  int t=blockIdx.x; __shared__ float sh[FH]; __shared__ float red[256];
  for(int i=threadIdx.x;i<FH;i+=256) sh[i]=h[t*FH+i];
  float p=0; for(int i=threadIdx.x;i<D;i+=256) p+=bf2f(u[t*D+i])*bf2f(gw[i]);
  red[threadIdx.x]=p; __syncthreads();
  float gs=0; for(int i=0;i<256;i++) gs+=red[i];
  float g=1.f/(1.f+expf(-gs));
  for(int d=threadIdx.x;d<D;d+=256){ float a=0; for(int f=0;f<FH;f++) a+=sh[f]*bf2f(wo[f*D+d]);
    float aa=y[t*D+d]+bf2f(u[t*D+d])*bf2f(dskip[d]);
    out[t*D+d]=f2bf(g*aa+(1.f-g)*a); }
}
extern "C" void kernel_launch(void* const* d_in, const int* in_sizes, int n_in, void* d_out, int out_size, void* d_ws, size_t ws_size, hipStream_t stream) {
  const u16* const* in=(const u16* const*)d_in;
  float* K=(float*)d_ws; float* y=K+D*L; float* z=y+L*D; float* h=z+L*F;
  k_kgen<<<D,256,0,stream>>>(in[1],in[2],in[3],in[4],in[5],in[6],in[7],K);
  k_conv<<<D,256,0,stream>>>(in[0],K,y);
  k_gemm1<<<L,256,0,stream>>>(in[0],in[10],z);
  k_gemm2<<<L,256,0,stream>>>(in[11],in[12],z,h);
  k_gemm3<<<L,256,0,stream>>>(h,in[13],in[0],in[8],in[14],y,(u16*)d_out);
}
```

```cpp
#include <hip/hip_runtime.h>
#include <hip/hip_bf16.h>

typedef unsigned short u16;
typedef unsigned u32;
typedef unsigned long long u64;
typedef short bf16x8 __attribute__((ext_vector_type(8)));
typedef short s16x4 __attribute__((ext_vector_type(4)));
typedef float f32x2 __attribute__((ext_vector_type(2)));
typedef float f32x4 __attribute__((ext_vector_type(4)));
typedef float f32x16 __attribute__((ext_vector_type(16)));
typedef unsigned u32x2 __attribute__((ext_vector_type(2)));
typedef unsigned u32x4 __attribute__((ext_vector_type(4)));
typedef __bf16 bf2_t __attribute__((ext_vector_type(2)));
#define LAS __attribute__((address_space(3)))
#define GAS __attribute__((address_space(1)))
#define DEV __device__ __forceinline__
#define SCHED() __builtin_amdgcn_sched_barrier(0)
#define CBAR() asm volatile("" ::: "memory")
#define WAITV(n) asm volatile("s_waitcnt vmcnt(%0)" ::"i"(n) : "memory")
#define WAITL0() asm volatile("s_waitcnt lgkmcnt(0)" ::: "memory")
#define HWBAR() __builtin_amdgcn_s_barrier()

constexpr int SEQ = 2048, DM = 640, NST = 192, FF = 1536, FH = 768;

constexpr u32 WS_EPOCH = 0;
constexpr u32 WS_FZ2 = 4096;
constexpr u32 WS_FA = WS_FZ2 + 256 * 256;
constexpr u32 WS_FH = WS_FA + 256 * 256;
constexpr u32 WS_STATS = WS_FH + 3 * 256 * 256;
constexpr u32 WS_Z2 = 1u << 20;
constexpr u32 WS_H = WS_Z2 + SEQ * FH * 2;
constexpr u32 WS_A = WS_H + SEQ * FH * 2;
constexpr u32 WS_END = WS_A + SEQ * DM * 2;
static_assert(WS_STATS + 32 * 4 * 8 * 256 <= WS_Z2, "ws map");

constexpr u32 GT_SZ = 10240, L_GT = 0;
constexpr u32 CS = 4160;
constexpr u32 SR_T = 4 * CS, SR_SZ = SR_T + 4608;
constexpr u32 L_SR = 4 * GT_SZ;
constexpr u32 NSLOT = 9, SLOT = 6144;
constexpr u32 L_RING = L_SR + 3 * SR_SZ;
constexpr u32 L_GW = L_RING + NSLOT * SLOT;
constexpr u32 L_FLG = L_GW + 1280;
constexpr u32 L_GATE = L_FLG + 256;
constexpr u32 L_TOTAL = L_GATE + 256;
static_assert(L_TOTAL <= 163840, "lds");
constexpr u32 L_RED = 0;
constexpr u32 L_WO = 65536;
constexpr u32 L_AT = L_WO + 81920;
static_assert(L_AT + 4 * 2560 <= L_GW, "late lds");
enum { F_CONS = 0, F_FULL = 4, F_XFREE = 8, F_UCNT = 12, F_Z2CNT = 13, F_ACNT = 14, F_WO01 = 15, F_WO2 = 16, F_H2CNT = 17, F_GW = 18 };

constexpr u32 SPIN_LDS = 1u << 17, SPIN_GLB = 1u << 14;

#define STAMP(k)

struct Params {
  const u16 *u, *Are, *Aim, *Bre, *Bim, *Cre, *Cim, *logdt, *dskip, *w_in, *w_sp, *b_sp, *w_out, *gate_w;
  u16* out;
  char* ws;
};

DEV float bf2f(u32 v) { return __uint_as_float(v << 16); }
DEV float bfhi(u32 v) { return __uint_as_float(v & 0xffff0000u); }
DEV u32 pk2(float a, float b) { return __builtin_bit_cast(u32, __builtin_convertvector(f32x2{a, b}, bf2_t)); }
DEV u32x4 mk_srd(const void* p, u32 bytes) { return u32x4{(u32)(size_t)p, (u32)((size_t)p >> 32) & 0xffffu, bytes, 0x00020000u}; }
DEV __amdgpu_buffer_rsrc_t mk_rsrc(const void* p, u32 bytes) { return __builtin_amdgcn_make_buffer_rsrc((void*)p, 0, bytes, 0x00020000); }
DEV void dma16(u32x4 srd, u32 voff, u32 soff, u32 dst) {
  asm volatile("s_mov_b32 m0, %2\n\ts_nop 0\n\tbuffer_load_dwordx4 %0, %1, %3 offen lds" ::"v"(voff), "s"(srd), "s"(dst), "s"(soff) : "memory");
}
template <class T> DEV LAS T* lp(u32 base, u32 off = 0) { return (LAS T*)((LAS char*)(size_t)base + off); }
DEV u32 opq(u32 x) { asm volatile("" : "+v"(x)); return x; }
DEV bf16x8 lds128(u32 base, u32 off = 0) { return *lp<bf16x8>(base, off); }
DEV s16x4 ldstr(u32 base, u32 off = 0) { return __builtin_amdgcn_ds_read_tr16_b64_v4i16(lp<s16x4>(base, off)); }
DEV bf16x8 cat8(s16x4 a, s16x4 b) { return __builtin_shufflevector(a, b, 0, 1, 2, 3, 4, 5, 6, 7); }
DEV u32 flag_ld(int k) { return __hip_atomic_load(lp<u32>(L_FLG + 4 * k), __ATOMIC_RELAXED, __HIP_MEMORY_SCOPE_WORKGROUP); }
DEV void flag_st(int k, u32 v) { CBAR(); __hip_atomic_store(lp<u32>(L_FLG + 4 * k), v, __ATOMIC_RELAXED, __HIP_MEMORY_SCOPE_WORKGROUP); CBAR(); }
DEV u32 flag_add(int k, int lane) {
  CBAR();
  u32 old = 0;
  if (lane == 0) old = __hip_atomic_fetch_add(lp<u32>(L_FLG + 4 * k), 1u, __ATOMIC_RELAXED, __HIP_MEMORY_SCOPE_WORKGROUP);
  old = __builtin_amdgcn_readfirstlane(old);
  CBAR();
  return old;
}
DEV u32 wait_flag_ge(int k, u32 want) {
  u32 v = 0;
#pragma nounroll
  for (u32 sp = 0; sp < SPIN_LDS; ++sp) {
    CBAR();
    v = __builtin_amdgcn_readfirstlane(flag_ld(k));
    if (v >= want) break;
    __builtin_amdgcn_s_sleep(1);
  }
  CBAR();
  return v;
}
DEV u32 gld_sc1(char* ws, u32 off) { return __hip_atomic_load((GAS u32*)(ws + off), __ATOMIC_RELAXED, __HIP_MEMORY_SCOPE_AGENT); }
DEV void gst_sc1(char* ws, u32 off, u32 v) { __hip_atomic_store((GAS u32*)(ws + off), v, __ATOMIC_RELAXED, __HIP_MEMORY_SCOPE_AGENT); }
DEV void wait_gflags(char* ws, u32 off, u32 tag) {
#pragma nounroll
  for (u32 sp = 0; sp < SPIN_GLB; ++sp) {
    CBAR();
    const u32 v = gld_sc1(ws, off);
    if (__all(v == tag)) break;
    __builtin_amdgcn_s_sleep(2);
  }
  CBAR();
}
DEV float gelu_f(float a) {
  const float k0 = -2.3022081999f, k1 = -0.1029432397f;
  const float e = __builtin_amdgcn_exp2f(a * __builtin_fmaf(a * a, k1, k0));
  return a * __builtin_amdgcn_rcpf(1.0f + e);
}
template <int CTRL> DEV float dppf(float x) { return __builtin_bit_cast(float, __builtin_amdgcn_update_dpp(0, __builtin_bit_cast(int, x), CTRL, 0xf, 0xf, false)); }
#define MFMA16(a, b, c) __builtin_amdgcn_mfma_f32_16x16x32_bf16(a, b, c, 0, 0, 0)
#define MFMA32(a, b, c) __builtin_amdgcn_mfma_f32_32x32x16_bf16(a, b, c, 0, 0, 0)

DEV f32x2 cmul(f32x2 x, f32x2 e, f32x2 er) { return __builtin_elementwise_fma(f32x2{x.y, x.y}, er, f32x2{x.x, x.x} * e); }
DEV f32x2 cexp2(float ar, float ai, float l) {
  const float m = __builtin_amdgcn_exp2f(ar * l), th = __builtin_amdgcn_fractf(ai * l);
  return f32x2{m * __builtin_amdgcn_cosf(th), m * __builtin_amdgcn_sinf(th)};
}

DEV void a_issue(u32x4 srd, u32 voff0, u32 voff1, u32 sbase, int bb, u32 buf) {
#pragma unroll
  for (int u = 0; u < 4; ++u) dma16(srd, (u & 1) ? voff1 : voff0, sbase + (u32)((32 * (bb & 1) + 16 * (u >> 1)) * 4096 + (bb >> 1) * 128), buf + u * 1024);
}
DEV void a_read(bf16x8 (&A)[8][4], u32 rd0, u32 rd1, int bb, u32 off) {
#pragma unroll
  for (int m = 0; m < 2; ++m) {
    A[2 * (bb >> 1)][2 * (bb & 1) + m] = lds128(rd0, off + m * 2048);
    A[2 * (bb >> 1) + 1][2 * (bb & 1) + m] = lds128(rd1, off + m * 2048);
  }
}

template <bool G>
DEV void tail(const Params& P, const bf16x8 (&A)[8][4], const u32 (&z1p)[6][2], float bsp, u32 tag, int w, int lane, int ti, int tj) {
  char* ws = P.ws;
  const __amdgpu_buffer_rsrc_t r_ws = mk_rsrc(ws, WS_END);
  const int c16 = lane & 15, q = lane >> 4;
  __builtin_amdgcn_s_setprio(0);
  wait_gflags(ws, WS_FZ2 + (u32)((tj * 32 + 4 * w + (lane & 3)) * 256), tag);
  constexpr int WIN = 10;
  const u32 zb = WS_Z2 + (u32)((tj * 3 * 64 + 8 * w) * 2048 + lane * 16);
  u32x4 win[WIN];
  auto zload = [&](int n) { win[n % WIN] = __builtin_amdgcn_raw_buffer_load_b128(r_ws, zb, (n >> 4) * 131072 + (n & 15) * 1024, 16); };
  const u32x4 s_wo = mk_srd(P.w_out, FH * DM * 2);
  u32 wov[5];
  auto wov_init = [&]() {
    int ln = lane;
    asm volatile("" : "+v"(ln));
#pragma unroll
    for (int u = 0; u < 5; ++u) { const int off = u * 1024 + ln * 16; wov[u] = (u32)((off / 160) * 1280 + (off % 160)); }
  };
  auto wo_issue = [&](int p, int jj, u32 dst) {
#pragma unroll
    for (int u = 0; u < 5; ++u) dma16(s_wo, wov[u], (u32)((96 * jj + 32 * p) * 1280 + 160 * tj), dst + (u32)(jj * 5120 + u * 1024));
  };
  auto reduce_publish = [&](int p) {
    f32x4 s[2] = {};
    const u32 rb = opq(L_RED + (u32)(2 * w * 1024 + lane * 16));
#pragma unroll
    for (int sw = 0; sw < 8; ++sw) {
      s[0] += *lp<f32x4>(rb, sw * 8192);
      s[1] += *lp<f32x4>(rb, sw * 8192 + 1024);
      if (p < 2 && (sw & 1)) asm volatile("" : "+v"(s[0]), "+v"(s[1])::"memory");
    }
    u32 o[4];
#pragma unroll
    for (int e = 0; e < 2; ++e)
#pragma unroll
      for (int k = 0; k < 2; ++k)
        o[2 * e + k] = pk2(bf2f(z1p[2 * p + e][k] & 0xffffu) * (s[e][2 * k] + bsp), bfhi(z1p[2 * p + e][k]) * (s[e][2 * k + 1] + bsp));
    __builtin_amdgcn_raw_buffer_store_b128(u32x4{o[0], o[1], o[2], o[3]}, r_ws, opq(lane * 16) + WS_H + (u32)((((ti * 4 + w) * 3 + p) * 8 + tj) * 1024), 0, 16);
  };
#pragma unroll
  for (int n = 0; n < WIN; ++n) zload(n);
  SCHED();
#pragma unroll
  for (int p = 0; p < 3; ++p) {
    f32x4 acc[4][2] = {};
#pragma unroll
    for (int ks = 0; ks < 8; ++ks) {
#pragma unroll
      for (int e = 0; e < 2; ++e) {
        const int n = p * 16 + ks * 2 + e;
        const bf16x8 zf = __builtin_bit_cast(bf16x8, win[n % WIN]);
#pragma unroll
        for (int m = 0; m < 4; ++m) acc[m][e] = MFMA16(zf, A[ks][m], acc[m][e]);
        SCHED();
        if (n + WIN < 48) zload(n + WIN);
        SCHED();
      }
      if (G && p > 0 && ks == 0) reduce_publish(p - 1);
    }
    if (p == 2) {
      wov_init();
      if (!G) { wo_issue(0, 2 * (w - 4), L_WO); wo_issue(0, 2 * (w - 4) + 1, L_WO); wo_issue(1, 2 * (w - 4), L_WO + 40960); wo_issue(1, 2 * (w - 4) + 1, L_WO + 40960); }
    }
    if (G) { if (p == 1) WAITV(WIN); if (p == 2) WAITV(0); }
    WAITL0();
    HWBAR();
    if (G && p > 0 && threadIdx.x == 0) gst_sc1(ws, WS_FH + (u32)((((p - 1) * 32 + ti) * 8 + tj) * 256), tag);
    const u32 wb = opq(L_RED + (u32)(w * 8192 + lane * 16));
#pragma unroll
    for (int m = 0; m < 4; ++m)
#pragma unroll
      for (int e = 0; e < 2; ++e) *lp<f32x4>(wb, (2 * m + e) * 1024) = acc[m][e];
    WAITL0();
    HWBAR();
  }

  if (G) {
    reduce_publish(2);
    WAITV(0);
    if (flag_add(F_H2CNT, lane) == 3u && lane == 0) gst_sc1(ws, WS_FH + (u32)(((2 * 32 + ti) * 8 + tj) * 256), tag);
    wait_flag_ge(F_H2CNT, 4);
    wo_issue(2, 2 * w, L_RED);
    wo_issue(2, 2 * w + 1, L_RED);
    WAITV(0);
    flag_add(F_WO2, lane);
  } else {
    const int rt = w - 4;
    const u32 at = L_AT + rt * 2560, atl = opq(at + lane * 16);
    const int a4 = (lane >> 2) & 3, p4 = lane & 3;
    wait_gflags(ws, WS_FA + (u32)((tj * 32 + (lane & 31)) * 256), tag);
    {
      const u32 ao = WS_A + (u32)((tj * 80 + (lane >> 1)) * 4096 + (ti * 64 + rt * 16) * 2 + (lane & 1) * 16);
      const u32x4 v0 = __builtin_amdgcn_raw_buffer_load_b128(r_ws, ao, 0, 16), v1 = __builtin_amdgcn_raw_buffer_load_b128(r_ws, ao, 32 * 4096, 16);
      u32x4 v2 = {};
      if (lane < 32) v2 = __builtin_amdgcn_raw_buffer_load_b128(r_ws, ao, 64 * 4096, 16);
      *lp<u32x4>(atl) = v0;
      *lp<u32x4>(atl, 1024) = v1;
      if (lane < 32) *lp<u32x4>(atl, 2048) = v2;
    }
    WAITV(0);
    flag_add(F_WO01, lane);
    f32x4 o[5];
    {
      const float gate = *lp<float>(L_GATE + (u32)((rt * 16 + c16) * 4));
      const u32 ab = opq(at + (u32)((4 * q + a4) * 32 + 8 * p4));
#pragma unroll
      for (int e = 0; e < 5; ++e) {
        const u32x2 v = __builtin_bit_cast(u32x2, ldstr(ab, 512 * e));
        o[e] = f32x4{gate * bf2f(v.x & 0xffffu), gate * bfhi(v.x), gate * bf2f(v.y & 0xffffu), gate * bfhi(v.y)};
      }
    }
    const u32 wrd = (u32)((4 * q + a4) * 160 + 8 * p4);
#pragma unroll
    for (int p = 0; p < 3; ++p) {
      wait_gflags(ws, WS_FH + (u32)(((p * 32 + ti) * 8 + (lane & 7)) * 256), tag);
      u32x4 hf[8];
#pragma unroll
      for (int jj = 0; jj < 8; ++jj) hf[jj] = __builtin_amdgcn_raw_buffer_load_b128(r_ws, WS_H + (u32)(((ti * 4 + rt) * 3 + p) * 8192 + lane * 16), jj * 1024, 16);
      if (p == 0) wait_flag_ge(F_WO01, 4);
      if (p == 2) wait_flag_ge(F_WO2, 4);
      const u32 wb = opq(wrd + (p == 2 ? L_RED : L_WO + p * 40960));
#pragma unroll
      for (int jj = 0; jj < 8; ++jj)
#pragma unroll
        for (int e = 0; e < 5; ++e) {
          const u32 ad = (u32)(jj * 5120 + 32 * e);
          o[e] = MFMA16(cat8(ldstr(wb, ad), ldstr(wb, ad + 2560)), __builtin_bit_cast(bf16x8, hf[jj]), o[e]);
        }
    }
    if (blockIdx.x == 0 && threadIdx.x == 256) gst_sc1(ws, WS_EPOCH, tag);
    const u32 ob = opq(at + (u32)(c16 * 160 + 8 * q));
#pragma unroll
    for (int e = 0; e < 5; ++e) *lp<u32x2>(ob, 32 * e) = u32x2{pk2(o[e][0], o[e][1]), pk2(o[e][2], o[e][3])};
    const __amdgpu_buffer_rsrc_t r_out = mk_rsrc(P.out, SEQ * DM * 2);
#pragma unroll
    for (int u = 0; u < 3; ++u) {
      if (u < 2 || lane < 32) {
        const u32x4 v = *lp<u32x4>(atl, u * 1024);
        __builtin_amdgcn_raw_buffer_store_b128(v, r_out, wov[u] + (u32)((ti * 64 + rt * 16) * 1280 + 160 * tj), 0, 0);
      }
    }
  }
}

__global__ __launch_bounds__(512, 2) void fused_kernel(Params P) {
  extern __shared__ __attribute__((aligned(16))) char smem[];
  const int lane = threadIdx.x & 63;
  const int w = __builtin_amdgcn_readfirstlane(threadIdx.x >> 6);
  const int b = blockIdx.x, xcd = b & 7, idx = b >> 3;
  const int ti = (xcd >> 1) * 8 + (idx >> 2), tj = (xcd & 1) * 4 + (idx & 3);
  const int c16 = lane & 15, q = lane >> 4;
  char* ws = P.ws;
  const __amdgpu_buffer_rsrc_t r_ws = mk_rsrc(ws, WS_END);
  const u32x4 s_wsp = mk_srd(P.w_sp, SEQ * SEQ * 2);
  const int r8 = lane >> 3, ch8 = lane & 7;
  const u32 sw0 = (u32)((ch8 ^ (r8 >> 1)) * 16), sw1 = (u32)((ch8 ^ (4 + (r8 >> 1))) * 16);
  const u32 rd0 = (u32)((c16 >> 3) * 1024 + (c16 & 7) * 128 + ((q ^ (c16 >> 1)) * 16));
  const u32 rd1 = rd0 ^ 64;
  const u32 a_v0 = (u32)(r8 * 4096) + sw0, a_v1 = (u32)((8 + r8) * 4096) + sw1;
  const u32 a_sb = (u32)(ti * 64 * 4096 + w * 512);

  if (w < 4) {
    const u32x4 s_x = mk_srd(P.u, SEQ * DM * 2);
    const u32 v0 = (u32)(r8 * 1280) + sw0, v1 = (u32)((8 + r8) * 1280) + sw1;
    const u32 sb = (u32)((ti * 64 + w * 16) * 1280);
#pragma unroll
    for (int cb = 0; cb < 5; ++cb) {
      dma16(s_x, v0, sb + cb * 128, L_GT + w * GT_SZ + cb * 2048);
      dma16(s_x, v1, sb + cb * 128, L_GT + w * GT_SZ + cb * 2048 + 1024);
    }
  }
  if (w == 4) *lp<u32>(L_FLG + 4 * lane) = 0;
  WAITL0();
  HWBAR();

  bf16x8 A[8][4];
  u32 z1p[6][2] = {};
  float bsp = 0.f;
  u32 tag;

  if (w < 4) {
    __builtin_amdgcn_s_setprio(2);
    const u32 bsp_raw = P.b_sp[ti * 64 + w * 16 + c16];
    const u32 ep_raw = gld_sc1(ws, WS_EPOCH);
    bf16x8 X[20];
    const u32 xr0 = opq(L_GT + w * GT_SZ + rd0), xr1 = opq(L_GT + w * GT_SZ + rd1);
    {
      const u32x4 s_x = mk_srd(P.u, SEQ * DM * 2);
      const u32 v0 = (u32)(r8 * 1280) + sw0, v1 = (u32)((8 + r8) * 1280) + sw1;
      const u32 sb = (u32)((ti * 64 + w * 16) * 1280) + 640;
      const u32 gt = L_GT + w * GT_SZ;
      WAITV(0);
#pragma unroll
      for (int cb = 0; cb < 5; ++cb) { X[2 * cb] = lds128(xr0, cb * 2048); X[2 * cb + 1] = lds128(xr1, cb * 2048); }
      WAITL0();
#pragma unroll
      for (int cb = 0; cb < 5; ++cb) {
        dma16(s_x, v0, sb + cb * 128, gt + cb * 2048);
        dma16(s_x, v1, sb + cb * 128, gt + cb * 2048 + 1024);
      }
    }
    const int a4 = (lane >> 2) & 3, p4 = lane & 3;
    const u32 wb = L_RING + (u32)((8 * q + a4) * 192 + (p4 >> 1) * 16 + (p4 & 1) * 8);
    const u32 wbe = opq(wb + 32 * (q & 1)), wbo = opq(wb + 32 * (1 - (q & 1))), gwb = opq(L_GW + q * 16);
    f32x4 acc2[6] = {}, acc1[6] = {}, accg = {};
    bf16x8 Wf[2][6];
    u32 full = 0;
    auto ring_read = [&](int n) {
      if (full <= (u32)n) full = wait_flag_ge(F_FULL, n + 1);
      const u32 so = (u32)((n % NSLOT) * SLOT);
#pragma unroll
      for (int e = 0; e < 6; ++e) {
        const u32 ad = so + 64 * (e >> 1);
        Wf[n & 1][e] = cat8(ldstr((e & 1) ? wbo : wbe, ad), ldstr((e & 1) ? wbo : wbe, ad + 768));
      }
      flag_st(F_CONS + w, n + 1);
    };
    ring_read(0);
#pragma unroll
    for (int n = 0; n < 20; ++n) {
      if (n == 9) {
        WAITV(0);
#pragma unroll
        for (int cb = 0; cb < 5; ++cb) { X[10 + 2 * cb] = lds128(xr0, cb * 2048); X[11 + 2 * cb] = lds128(xr1, cb * 2048); }
        flag_st(F_XFREE + w, 1);
      }
      if (n + 1 < 20) ring_read(n + 1);
      SCHED();
#pragma unroll
      for (int e = 0; e < 6; ++e) acc2[e] = MFMA16(X[n], Wf[n & 1][e], acc2[e]);
      SCHED();
    }
    tag = __builtin_amdgcn_readfirstlane(ep_raw) + 1u;
    bsp = bf2f(bsp_raw);
    {
      float s1[4] = {}, s2[4] = {};
#pragma unroll
      for (int e = 0; e < 6; ++e)
#pragma unroll
        for (int r = 0; r < 4; ++r) { const float g = gelu_f(acc2[e][r]); acc2[e][r] = g; s1[r] += g; s2[r] = __builtin_fmaf(g, g, s2[r]); }
#pragma unroll
      for (int r = 0; r < 4; ++r) {
        s1[r] += dppf<0x128>(s1[r]); s2[r] += dppf<0x128>(s2[r]);
        s1[r] += dppf<0x124>(s1[r]); s2[r] += dppf<0x124>(s2[r]);
        s1[r] += dppf<0x122>(s1[r]); s2[r] += dppf<0x122>(s2[r]);
        s1[r] += dppf<0x121>(s1[r]); s2[r] += dppf<0x121>(s2[r]);
      }
      const float a1 = (p4 & 2) ? ((p4 & 1) ? s1[3] : s1[2]) : ((p4 & 1) ? s1[1] : s1[0]);
      const float a2 = (p4 & 2) ? ((p4 & 1) ? s2[3] : s2[2]) : ((p4 & 1) ? s2[1] : s2[0]);
      if (c16 < 4)
        __builtin_amdgcn_raw_buffer_store_b128(u32x4{__float_as_uint(a1), tag, __float_as_uint(a2), tag}, r_ws,
                                               WS_STATS + (u32)(((ti * 4 + w) * 8 + tj) * 256 + (4 * q + c16) * 16), 0, 16);
    }
    wait_flag_ge(F_GW, 1);
    ring_read(20);
#pragma unroll
    for (int n = 20; n < 40; ++n) {
      if (n + 1 < 40) ring_read(n + 1);
      const bf16x8 gwf = lds128(gwb, (n - 20) * 64);
      SCHED();
#pragma unroll
      for (int e = 0; e < 6; ++e) acc1[e] = MFMA16(Wf[n & 1][e], X[n - 20], acc1[e]);
      accg = MFMA16(gwf, X[n - 20], accg);
      SCHED();
    }
    {
      const u32 so = WS_STATS + (u32)((ti * 4 + w) * 2048 + (c16 >> 2) * 256 + (4 * q + p4) * 16);
      u32x4 g0, g1;
#pragma nounroll
      for (u32 sp = 0; sp < SPIN_GLB; ++sp) {
        CBAR();
        g0 = __builtin_amdgcn_raw_buffer_load_b128(r_ws, so, 0, 16);
        g1 = __builtin_amdgcn_raw_buffer_load_b128(r_ws, so + 1024, 0, 16);
        if (__all(g0.y == tag && g0.w == tag && g1.y == tag && g1.w == tag)) break;
        __builtin_amdgcn_s_sleep(2);
      }
      CBAR();
      float t1 = __uint_as_float(g0.x) + __uint_as_float(g1.x), t2 = __uint_as_float(g0.z) + __uint_as_float(g1.z);
      t1 += dppf<0x128>(t1); t2 += dppf<0x128>(t2);
      t1 += dppf<0x124>(t1); t2 += dppf<0x124>(t2);
      const float mean = t1 * (1.0f / FH);
      const float var = fmaxf(__builtin_fmaf(-mean, mean, t2 * (1.0f / FH)), 0.f);
      const float rs = __builtin_amdgcn_rsqf(var + 1e-5f), mo = -mean * rs;
      const float rr[4] = {dppf<0x00>(rs), dppf<0x55>(rs), dppf<0xAA>(rs), dppf<0xFF>(rs)};
      const float mm[4] = {dppf<0x00>(mo), dppf<0x55>(mo), dppf<0xAA>(mo), dppf<0xFF>(mo)};
      const u32 zo = WS_Z2 + (u32)(((tj * 3 * 64 + 2 * ti + (w >> 1)) * 2 + (q & 1)) * 1024 + (c16 + 16 * (2 * (w & 1) + (q >> 1))) * 16);
#pragma unroll
      for (int p = 0; p < 3; ++p) {
        u32 x[2], y[2];
#pragma unroll
        for (int k = 0; k < 2; ++k) {
          x[k] = pk2(__builtin_fmaf(acc2[2 * p][2 * k], rr[2 * k], mm[2 * k]), __builtin_fmaf(acc2[2 * p][2 * k + 1], rr[2 * k + 1], mm[2 * k + 1]));
          y[k] = pk2(__builtin_fmaf(acc2[2 * p + 1][2 * k], rr[2 * k], mm[2 * k]), __builtin_fmaf(acc2[2 * p + 1][2 * k + 1], rr[2 * k + 1], mm[2 * k + 1]));
        }
        const auto s0 = __builtin_amdgcn_permlane16_swap(x[0], y[0], false, false);
        const auto s1 = __builtin_amdgcn_permlane16_swap(x[1], y[1], false, false);
        __builtin_amdgcn_raw_buffer_store_b128(u32x4{s0[0], s1[0], s0[1], s1[1]}, r_ws, zo + p * 131072, 0, 16);
      }
    }
    {
      const float gate = __builtin_amdgcn_rcpf(1.0f + __builtin_amdgcn_exp2f(accg[0] * -1.4426950409f));
      if (q == 0) *lp<float>(L_GATE + (u32)((w * 16 + c16) * 4)) = gate;
      const float og = 1.0f - gate;
#pragma unroll
      for (int e = 0; e < 6; ++e) {
        z1p[e][0] = pk2(gelu_f(acc1[e][0]) * og, gelu_f(acc1[e][1]) * og);
        z1p[e][1] = pk2(gelu_f(acc1[e][2]) * og, gelu_f(acc1[e][3]) * og);
        asm volatile("" : "+v"(z1p[e][0]), "+v"(z1p[e][1]));
      }
    }
    WAITV(0);
    if (flag_add(F_Z2CNT, lane) == 3u && lane == 0) gst_sc1(ws, WS_FZ2 + (u32)((tj * 32 + ti) * 256), tag);
    wait_flag_ge(F_Z2CNT, 4);
    {
      const u32 bf = L_RING + w * 12288, ar0 = opq(bf + rd0), ar1 = opq(bf + rd1);
      a_issue(s_wsp, a_v0, a_v1, a_sb, 0, bf);
      a_issue(s_wsp, a_v0, a_v1, a_sb, 1, bf + 4096);
      a_issue(s_wsp, a_v0, a_v1, a_sb, 2, bf + 8192);
#pragma unroll
      for (int bb = 0; bb < 8; ++bb) {
        if (bb < 6) WAITV(8); else if (bb == 6) WAITV(4); else WAITV(0);
        a_read(A, ar0, ar1, bb, (bb % 3) * 4096);
        WAITL0();
        if (bb + 3 < 8) a_issue(s_wsp, a_v0, a_v1, a_sb, bb + 3, bf + (bb % 3) * 4096);
      }
    }
    tail<true>(P, A, z1p, bsp, tag, w, lane, ti, tj);
    return;
  } else if (w == 7) {
    __builtin_amdgcn_s_setprio(3);
    const u32x4 s_w = mk_srd(P.w_in, DM * FF * 2);
    u32 v[6];
#pragma unroll
    for (int u = 0; u < 6; ++u) {
      const int off = u * 1024 + lane * 16, row = off / 192, ch = (off % 192) >> 4;
      v[u] = (u32)(row * 3072 + (ch ^ (2 * ((row >> 3) & 1))) * 16);
    }
    u32 slot = 0, pub = 0;
#pragma nounroll
    for (u32 n = 0; n < 40; ++n) {
      if (n >= NSLOT) {
#pragma nounroll
        for (u32 sp = 0; sp < SPIN_LDS; ++sp) {
          CBAR();
          const u32x4 c = *(volatile LAS u32x4*)(size_t)(L_FLG + 4 * F_CONS);
          const u32 mn = __builtin_amdgcn_readfirstlane(min(min(c.x, c.y), min(c.z, c.w)));
          if (mn + NSLOT > n) break;
          if (pub < n) {
            const u32 d = n - pub - 1;
            if (d >= 8) WAITV(48); else if (d == 7) WAITV(42); else if (d == 6) WAITV(36); else if (d == 5) WAITV(30); else if (d == 4) WAITV(24);
            else if (d == 3) WAITV(18); else if (d == 2) WAITV(12); else if (d == 1) WAITV(6); else WAITV(0);
            ++pub;
            flag_st(F_FULL, pub);
          } else {
            __builtin_amdgcn_s_sleep(1);
          }
        }
      }
      const u32 sb = (n < 20 ? (u32)(FH * 2) + n * (32 * 3072) : (n - 20) * (32 * 3072)) + (u32)(tj * 192);
#pragma unroll
      for (int u = 0; u < 6; ++u) dma16(s_w, v[u], sb, L_RING + slot * SLOT + u * 1024);
      slot = slot + 1 == NSLOT ? 0 : slot + 1;
    }
#pragma nounroll
    while (pub < 40) {
      const u32 d = 40 - pub - 1;
      if (d >= 8) WAITV(48); else if (d == 7) WAITV(42); else if (d == 6) WAITV(36); else if (d == 5) WAITV(30); else if (d == 4) WAITV(24);
      else if (d == 3) WAITV(18); else if (d == 2) WAITV(12); else if (d == 1) WAITV(6); else WAITV(0);
      ++pub;
      flag_st(F_FULL, pub);
    }
    __builtin_amdgcn_s_setprio(0);
    tag = __builtin_amdgcn_readfirstlane(gld_sc1(ws, WS_EPOCH)) + 1u;
  } else {
    const int sv = w - 4;
    const int c0 = 80 * xcd + (5 * idx) / 2, nch = 2 + (idx & 1);
    const bool has = sv < nch;
    const int chn = has ? c0 + sv : c0;
    const u32 sr = L_SR + sv * SR_SZ;
    u32 pr[6][3];
    {
      const u16* const src[6] = {P.Are, P.Aim, P.Bre, P.Bim, P.Cre, P.Cim};
#pragma unroll
      for (int a = 0; a < 6; ++a)
#pragma unroll
        for (int ps = 0; ps < 3; ++ps) pr[a][ps] = src[a][chn * NST + ps * 64 + lane];
    }
    const float ldt = bf2f(P.logdt[chn]), dsk = bf2f(P.dskip[chn]);
    const u32 ep_raw = gld_sc1(ws, WS_EPOCH);
    u32x4 gw0 = {}, gw1 = {};
    if (sv == 0) {
      const __amdgpu_buffer_rsrc_t r_gw = mk_rsrc(P.gate_w, DM * 2);
      gw0 = __builtin_amdgcn_raw_buffer_load_b128(r_gw, lane * 16, 0, 0);
      gw1 = __builtin_amdgcn_raw_buffer_load_b128(r_gw, lane * 16 + 1024, 0, 0);
    }
    const int cw = min(c0 & ~1, DM - 4);
    u32x2 ug[2][8];
    {
      const __amdgpu_buffer_rsrc_t r_u = mk_rsrc(P.u, SEQ * DM * 2);
      const u32 vo = (u32)((sv * 64 + lane) * 8 * 1280 + cw * 2);
#pragma unroll
      for (int r = 0; r < 8; ++r) ug[0][r] = __builtin_amdgcn_raw_buffer_load_b64(r_u, vo, r * 1280, 0);
      if (sv == 2) {
#pragma unroll
        for (int r = 0; r < 8; ++r) ug[1][r] = __builtin_amdgcn_raw_buffer_load_b64(r_u, vo, 512 * 1280 + r * 1280, 0);
      }
    }
    f32x16 kacc = {};
    if (has) {
      const float dt = bf2f(pk2(expf(ldt), 0.f) & 0xffffu);
      const u32 wr = opq(sr + (u32)(lane * 4)), rdp = opq(sr + (u32)((lane & 31) * 272 + (lane >> 5) * 16));
#pragma unroll
      for (int ps = 0; ps < 3; ++ps) {
        const float ar = (dt * bf2f(pr[0][ps])) * 1.4426950409f, ai = (dt * bf2f(pr[1][ps])) * 0.15915494309f;
        const float br = dt * bf2f(pr[2][ps]), bi = dt * bf2f(pr[3][ps]), cr = bf2f(pr[4][ps]), ci = bf2f(pr[5][ps]);
        const f32x2 e1 = cexp2(ar, -ai, 1.f), e256 = cexp2(ar, -ai, 256.f), e8 = cexp2(ar, ai, 8.f);
        const f32x2 e1r = {-e1.y, e1.x}, e256r = {-e256.y, e256.x}, e8r = {-e8.y, e8.x};
        f32x2 pv[8];
        pv[0] = f32x2{__builtin_fmaf(cr, br, -ci * bi), -__builtin_fmaf(cr, bi, ci * br)};
#pragma unroll
        for (int c = 1; c < 8; ++c) pv[c] = cmul(pv[c - 1], e1, e1r);
#pragma unroll
        for (int c = 0; c < 8; ++c) *lp<u32>(wr, c * 272) = pk2(pv[c].x, pv[c].y);
        f32x2 t0 = cmul(pv[0], e256, e256r), t2 = cmul(pv[2], e256, e256r), t4 = cmul(pv[4], e256, e256r), t6 = cmul(pv[6], e256, e256r);
        *lp<u32>(wr, 8 * 272) = pk2(t0.x, t0.y); *lp<u32>(wr, 9 * 272) = pk2(t2.x, t2.y);
        *lp<u32>(wr, 10 * 272) = pk2(t4.x, t4.y); *lp<u32>(wr, 11 * 272) = pk2(t6.x, t6.y);
        t0 = cmul(t0, e256, e256r); t4 = cmul(t4, e256, e256r);
        *lp<u32>(wr, 12 * 272) = pk2(t0.x, t0.y); *lp<u32>(wr, 13 * 272) = pk2(t4.x, t4.y);
        t0 = cmul(t0, e256, e256r); t4 = cmul(t4, e256, e256r);
        *lp<u32>(wr, 14 * 272) = pk2(t0.x, t0.y); *lp<u32>(wr, 15 * 272) = pk2(t4.x, t4.y);
#pragma unroll
        for (int a = 0; a < 5; ++a) { t0 = cmul(t0, e256, e256r); *lp<u32>(wr, (16 + a) * 272) = pk2(t0.x, t0.y); }
        bf16x8 pf[8];
#pragma unroll
        for (int kk = 0; kk < 8; ++kk) pf[kk] = lds128(rdp, kk * 32);
        const f32x2 e16 = cmul(e8, e8, e8r), e16r = {-e16.y, e16.x};
        f32x2 qa = {1.f, 0.f}, qb = e8;
#pragma unroll
        for (int bq = 0; bq < 32; bq += 2) {
          *lp<u32>(wr, bq * 272) = pk2(qa.x, qa.y);
          *lp<u32>(wr, (bq + 1) * 272) = pk2(qb.x, qb.y);
          if (bq + 2 < 32) { qa = cmul(qa, e16, e16r); qb = cmul(qb, e16, e16r); }
        }
#pragma unroll
        for (int kk = 0; kk < 8; ++kk) kacc = MFMA32(pf[kk], lds128(rdp, kk * 32), kacc);
      }
    }
    if (sv == 0) {
      *lp<u32x4>(L_GW + lane * 16) = gw0;
      if (lane < 16) *lp<u32x4>(L_GW + 1024 + lane * 16) = gw1;
      flag_st(F_GW, 1);
    }
#pragma unroll
    for (int g = 0; g < 2; ++g) {
      if (g == 0 || sv == 2) {
        const int m = (sv + g) * 64 + lane;
#pragma unroll
        for (int k = 0; k < 3; ++k) {
          const int e = c0 + k - cw;
          const u32 sel = (e & 1) ? 0x03020706u : 0x01000504u;
          u32 d[4];
#pragma unroll
          for (int t = 0; t < 4; ++t) {
            const u32 hi = (e & 2) ? ug[g][7 - 2 * t].y : ug[g][7 - 2 * t].x, lo = (e & 2) ? ug[g][6 - 2 * t].y : ug[g][6 - 2 * t].x;
            d[t] = __builtin_amdgcn_perm(hi, lo, sel);
          }
          if (k < nch) *lp<u32x4>(L_SR + k * SR_SZ + 3 * CS + (u32)((255 - m) * 16)) = u32x4{d[0], d[1], d[2], d[3]};
        }
      }
    }
    flag_add(F_UCNT, lane);
    tag = __builtin_amdgcn_readfirstlane(ep_raw) + 1u;
    const u32 abf = L_GT + sv * GT_SZ;
    if (has) {
      const u32 T = sr + SR_T, CB = sr;
      const int bq = lane & 31, hh = lane >> 5;
      if (lane == 0) kacc[0] += dsk;
      *lp<u32x2>(T + lane * 8) = u32x2{0u, 0u};
      *lp<u32x2>(T + 512 + (u32)(bq * 16 + hh * 8)) = u32x2{pk2(kacc[0], kacc[1]), pk2(kacc[2], kacc[3])};
      *lp<u32>(CB + (u32)(hh ? 256 + 4 * bq : 8 * bq)) = pk2(kacc[4], kacc[5]);
      *lp<u32>(CB + (u32)(hh ? 384 + 4 * bq : 8 * bq + 4)) = pk2(kacc[6], kacc[7]);
#pragma unroll
      for (int r = 0; r < 4; ++r) *lp<u16>(CB + (u32)(512 + 64 * (4 * hh + r) + 2 * bq)) = (u16)pk2(kacc[8 + r], 0.f);
      {
        const u32x2 v = *lp<u32x2>(CB + (u32)(8 * bq));
        const u32 n4 = *lp<u16>(CB + (u32)(8 * bq + 8));
        const u32x4 o = {__builtin_amdgcn_perm(v.x, v.x, 0x01000100u), __builtin_amdgcn_perm(v.y, v.x, 0x05040302u),
                         __builtin_amdgcn_perm(v.y, v.y, 0x01000100u), __builtin_amdgcn_perm(n4, v.y, 0x05040302u)};
        if (hh == 0) *lp<u32x4>(T + 1024 + (u32)(bq * 16)) = o;
      }
      {
        const u32 v = *lp<u32>(CB + (u32)(256 + 4 * lane));
        const u32 n2 = *lp<u16>(CB + (u32)(256 + 4 * lane + 4));
        *lp<u32x4>(T + 1536 + (u32)(lane * 16)) = u32x4{__builtin_amdgcn_perm(v, v, 0x01000100u), v, __builtin_amdgcn_perm(v, v, 0x03020302u), n2 * 0x10001u};
      }
#pragma unroll
      for (int g = 0; g < 2; ++g) {
        const u32 v0 = *lp<u16>(CB + (u32)(512 + 2 * (g * 64 + lane))), v1 = *lp<u16>(CB + (u32)(514 + 2 * (g * 64 + lane)));
        *lp<u32x4>(T + 2560 + (u32)((g * 64 + lane) * 16)) = u32x4{v0 * 0x10001u, v0 * 0x10001u, ((lane & 1) ? v1 : v0) | (v1 << 16), v1 * 0x10001u};
      }
      if (lane < 16) *lp<u32x4>(sr + (u32)((lane >> 2) * CS + 4096 + (lane & 3) * 16)) = u32x4{0u, 0u, 0u, 0u};
      wait_flag_ge(F_UCNT, 3);
      const u32 srl = opq(sr + lane * 16);
#pragma unroll
      for (int g = 0; g < 4; ++g) {
        const u32 o = (u32)(g * 1024);
        const u32x4 d = *lp<u32x4>(srl, 3 * CS + o);
        const u32x2 n = *lp<u32x2>(srl, 3 * CS + o + 16);
        const u32 a0 = __builtin_amdgcn_alignbit(d.y, d.x, 16), a1 = __builtin_amdgcn_alignbit(d.z, d.y, 16), a2 = __builtin_amdgcn_alignbit(d.w, d.z, 16),
                  a3 = __builtin_amdgcn_alignbit(n.x, d.w, 16), a4 = __builtin_amdgcn_alignbit(n.y, n.x, 16);
        *lp<u32x4>(srl, 2 * CS + o) = u32x4{a0, a1, a2, a3};
        *lp<u32x4>(srl, CS + o) = u32x4{d.y, d.z, d.w, n.x};
        *lp<u32x4>(srl, o) = u32x4{a1, a2, a3, a4};
      }
      wait_flag_ge(F_XFREE + sv, 1);
      const u32 ab = opq(sr + (u32)((c16 & 3) * CS + 24 - 8 * (c16 >> 2) + 16 * q)), ab8 = opq(ab + 8), bb_ = opq(T + (u32)(32 * c16 + 16 * q));
      const u32 ar0 = opq(abf + rd0), ar1 = opq(abf + rd1);
      auto ldA = [&](int p, int r) { const u32 ad = (u32)(4032 - 512 * p - 64 * r); return cat8(*lp<s16x4>(ab, ad), *lp<s16x4>(ab8, ad)); };
      auto ldB = [&](int m, int r) { return lds128(bb_, (u32)(512 * (m + 1) - 64 * r - 32)); };
      f32x4 y[8] = {};
      bf16x8 fa[8], fb[8];
#pragma unroll
      for (int p = 7; p >= 0; --p) { fa[p] = ldA(p, 0); fb[7 - p] = ldB(7 - p, 0); }
      a_issue(s_wsp, a_v0, a_v1, a_sb, 0, abf);
      a_issue(s_wsp, a_v0, a_v1, a_sb, 1, abf + 4096);
#pragma unroll
      for (int r = 0; r < 8; ++r) {
#pragma unroll
        for (int p = 7; p >= 0; --p) {
#pragma unroll
          for (int m = 0; m + p < 8; ++m) {
            y[p + m] = MFMA16(fa[p], fb[m], y[p + m]);
            if (p == 0 && r < 7) { SCHED(); fb[m] = ldB(m, r + 1); SCHED(); }
          }
          if (r < 7) { SCHED(); fa[p] = ldA(p, r + 1); SCHED(); }
          if (p == 3 && r >= 1) {
            if (r < 7) WAITV(4); else WAITV(0);
            a_read(A, ar0, ar1, r - 1, ((r - 1) & 1) * 4096);
            if (r == 7) a_read(A, ar0, ar1, 7, 4096);
          }
          if (p == 1 && r >= 1 && r < 7) { WAITL0(); a_issue(s_wsp, a_v0, a_v1, a_sb, r + 1, abf + ((r - 1) & 1) * 4096); }
        }
      }
      const u32 ao = WS_A + (u32)(chn * 4096 + (q & 1) * 512 + c16 * 32 + (q >> 1) * 16);
#pragma unroll
      for (int c = 0; c < 8; c += 2) {
        const auto s0 = __builtin_amdgcn_permlane16_swap(pk2(y[c][0], y[c][1]), pk2(y[c + 1][0], y[c + 1][1]), false, false);
        const auto s1 = __builtin_amdgcn_permlane16_swap(pk2(y[c][2], y[c][3]), pk2(y[c + 1][2], y[c + 1][3]), false, false);
        __builtin_amdgcn_raw_buffer_store_b128(u32x4{s0[0], s1[0], s0[1], s1[1]}, r_ws, ao + c * 512, 0, 16);
      }
      WAITV(0);
      if (flag_add(F_ACNT, lane) == (u32)(nch - 1) && lane == 0) gst_sc1(ws, WS_FA + (u32)((xcd * 32 + idx) * 256), tag);
      tail<false>(P, A, z1p, bsp, tag, w, lane, ti, tj);
      return;
    } else {
      wait_flag_ge(F_FULL, 30);
    }
  }
  {
    const u32 abf = L_GT + (w - 4) * GT_SZ, ar0 = opq(abf + rd0), ar1 = opq(abf + rd1);
    wait_flag_ge(F_XFREE + w - 4, 1);
    a_issue(s_wsp, a_v0, a_v1, a_sb, 0, abf);
    a_issue(s_wsp, a_v0, a_v1, a_sb, 1, abf + 4096);
#pragma unroll
    for (int bb = 0; bb < 8; ++bb) {
      if (bb < 7) WAITV(4); else WAITV(0);
      a_read(A, ar0, ar1, bb, (bb & 1) * 4096);
      WAITL0();
      if (bb + 2 < 8) a_issue(s_wsp, a_v0, a_v1, a_sb, bb + 2, abf + (bb & 1) * 4096);
    }
    tail<false>(P, A, z1p, bsp, tag, w, lane, ti, tj);
  }
}

extern "C" void kernel_launch(void* const* d_in, const int* in_sizes, int n_in, void* d_out, int out_size, void* d_ws, size_t ws_size,
                              hipStream_t stream) {
  Params P;
  P.u = (const u16*)d_in[0]; P.Are = (const u16*)d_in[1]; P.Aim = (const u16*)d_in[2]; P.Bre = (const u16*)d_in[3];
  P.Bim = (const u16*)d_in[4]; P.Cre = (const u16*)d_in[5]; P.Cim = (const u16*)d_in[6]; P.logdt = (const u16*)d_in[7];
  P.dskip = (const u16*)d_in[8]; P.w_in = (const u16*)d_in[10]; P.w_sp = (const u16*)d_in[11]; P.b_sp = (const u16*)d_in[12];
  P.w_out = (const u16*)d_in[13]; P.gate_w = (const u16*)d_in[14]; P.out = (u16*)d_out; P.ws = (char*)d_ws;
  (void)hipFuncSetAttribute((const void*)fused_kernel, hipFuncAttributeMaxDynamicSharedMemorySize, L_TOTAL);
  fused_kernel<<<256, 512, L_TOTAL, stream>>>(P);
}
```

```cpp
#include <hip/hip_runtime.h>
#include <hip/hip_bf16.h>

typedef unsigned short u16;
typedef unsigned u32;
typedef unsigned long long u64;
typedef short bf16x8 __attribute__((ext_vector_type(8)));
typedef short s16x4 __attribute__((ext_vector_type(4)));
typedef float f32x2 __attribute__((ext_vector_type(2)));
typedef float f32x4 __attribute__((ext_vector_type(4)));
typedef float f32x16 __attribute__((ext_vector_type(16)));
typedef unsigned u32x2 __attribute__((ext_vector_type(2)));
typedef unsigned u32x4 __attribute__((ext_vector_type(4)));
typedef __bf16 bf2_t __attribute__((ext_vector_type(2)));
typedef _Float16 h2_t __attribute__((ext_vector_type(2)));
typedef _Float16 h8_t __attribute__((ext_vector_type(8)));
#define LAS __attribute__((address_space(3)))
#define DEV __device__ __forceinline__
#define SCHED() __builtin_amdgcn_sched_barrier(0)
#define CBAR() asm volatile("" ::: "memory")
#define WAITV(n) asm volatile("s_waitcnt vmcnt(%0)" ::"i"(n) : "memory")
#define WAITL0() asm volatile("s_waitcnt lgkmcnt(0)" ::: "memory")
#define HWBAR() __builtin_amdgcn_s_barrier()

constexpr int SEQ = 2048, DM = 640, NST = 192, FF = 1536, FH = 768;

constexpr u32 WS_EPOCH = 0;
constexpr u32 WS_FZ2 = 4096;
constexpr u32 WS_FUT = WS_FZ2 + 256 * 256;
constexpr u32 WS_FA = WS_FUT + 256 * 256;
constexpr u32 WS_FH = WS_FA + 256 * 256;
constexpr u32 WS_STATS = WS_FH + 3 * 256 * 256;
constexpr u32 WS_Z2 = 1u << 20;
constexpr u32 WS_H = WS_Z2 + SEQ * FH * 2;
constexpr u32 WS_A = WS_H + SEQ * FH * 2;
constexpr u32 WS_UT = WS_A + SEQ * DM * 2;
constexpr u32 WS_END = WS_UT + SEQ * DM * 2;
static_assert(WS_STATS + 32 * 4 * 8 * 256 <= WS_Z2, "ws map");

constexpr u32 CHUNK = 12288, NSLOT = 7;
constexpr u32 CS = 4160;
constexpr u32 SR_T = 4 * CS, SR_SZ = SR_T + 4608;
constexpr u32 L_SR = 0;
constexpr u32 L_RING = L_SR + 3 * SR_SZ;
constexpr u32 L_GX = L_RING + NSLOT * CHUNK + 8192;
constexpr u32 L_GW = L_GX + 4096;
constexpr u32 L_FLG = L_GW + 1280;
constexpr u32 L_GATE = L_FLG + 256;
constexpr u32 L_TOTAL = L_GATE + 256;
static_assert(L_TOTAL <= 163840, "lds");
constexpr u32 L_RED = 0;
constexpr u32 L_WO = 65536;
constexpr u32 L_AT = L_WO + 81920;
static_assert(L_AT + 12800 <= L_GW, "late lds");
enum { F_CH = 0, F_EX = 4, F_Z2CNT = 8, F_ACNT = 9, F_WO01 = 10, F_WO2 = 11, F_H2CNT = 12, F_FULL = 13, F_AT = 14, F_GO = 15 };
#define GPRIO 2
#define SPRIO 0
#define LPRIO 1
#define PIT 4

#define STAMP(k)
#define TSARG
#define TSPASS
#define TSFLUSH()

struct Params {
  const u16 *u, *Are, *Aim, *Bre, *Bim, *Cre, *Cim, *logdt, *dskip, *w_in, *w_sp, *b_sp, *w_out, *gate_w;
  u16* out;
  char* ws;
};

typedef const __attribute__((address_space(4))) Params* KP;
DEV KP kargs() {
  KP p = (KP)__builtin_amdgcn_kernarg_segment_ptr();
  asm volatile("" : "+s"(p));
  return p;
}
DEV float bf2f(u32 v) { return __uint_as_float(v << 16); }
DEV float bfhi(u32 v) { return __uint_as_float(v & 0xffff0000u); }
DEV u32 pk2(float a, float b) { return __builtin_bit_cast(u32, __builtin_convertvector(f32x2{a, b}, bf2_t)); }
DEV u32x4 mk_srd(const void* p, u32 bytes) { return u32x4{(u32)(size_t)p, (u32)((size_t)p >> 32) & 0xffffu, bytes, 0x00020000u}; }
DEV __amdgpu_buffer_rsrc_t mk_rsrc(const void* p, u32 bytes) { return __builtin_amdgcn_make_buffer_rsrc((void*)p, 0, bytes, 0x00020000); }
DEV void dma16(u32x4 srd, u32 voff, u32 soff, u32 dst) {
  asm volatile("s_mov_b32 m0, %2\n\ts_nop 0\n\tbuffer_load_dwordx4 %0, %1, %3 offen lds" ::"v"(voff), "s"(srd), "s"(dst), "s"(soff) : "memory");
}
DEV void m0_set(u32 dst) { asm volatile("s_mov_b32 m0, %0" ::"s"(dst) : "memory"); }
template <int OFF> DEV void dma16o(u32x4 srd, u32 voff, u32 soff) {
  asm volatile("buffer_load_dwordx4 %0, %1, %2 offen offset:%3 lds" ::"v"(voff), "s"(srd), "s"(soff), "i"(OFF) : "memory");
}
DEV void bload(u32x4& d, u32x4 srd, u32 voff, u32 soff) {
  asm volatile("buffer_load_dwordx4 %0, %1, %2, %3 offen" : "=v"(d) : "v"(voff), "s"(srd), "s"(soff) : "memory");
}
DEV void bload_sc1(u32x4& d, u32x4 srd, u32 voff, u32 soff) {
  asm volatile("buffer_load_dwordx4 %0, %1, %2, %3 offen sc1" : "=v"(d) : "v"(voff), "s"(srd), "s"(soff) : "memory");
}
#define WAITV_PIN(n, x) asm volatile("s_waitcnt vmcnt(%1)" : "+v"(x) : "i"(n) : "memory")
template <class T> DEV LAS T* lp(u32 base, u32 off = 0) { return (LAS T*)((LAS char*)(size_t)base + off); }
DEV u32 opq(u32 x) { asm volatile("" : "+v"(x)); return x; }
DEV bf16x8 lds128(u32 base, u32 off = 0) { return *lp<bf16x8>(base, off); }
DEV s16x4 ldstr(u32 base, u32 off = 0) { return __builtin_amdgcn_ds_read_tr16_b64_v4i16(lp<s16x4>(base, off)); }
DEV bf16x8 cat8(s16x4 a, s16x4 b) { return __builtin_shufflevector(a, b, 0, 1, 2, 3, 4, 5, 6, 7); }
DEV bf16x8 as8(u32x4 v) { return __builtin_bit_cast(bf16x8, v); }

DEV u32 faddr(int k) { return L_FLG + 4 * k; }
DEV void flag_st(u32 addr, u32 v) { asm volatile("ds_write_b32 %0, %1" ::"v"(addr), "v"(v) : "memory"); }
DEV void flag_inc(u32 addr) {
  asm volatile("s_mov_b64 exec, 1\n\tds_add_u32 %0, %1\n\ts_mov_b64 exec, -1" ::"v"(addr), "v"(1u) : "memory");
}
DEV void flag_pre(u32& pre, u32 addr) { asm volatile("ds_read_b32 %0, %1" : "=v"(pre) : "v"(addr) : "memory"); }
template <int SLEEP = 1> DEV void flag_wait_pre(u32 pre, u32 addr, u32 want) {
  u32 t, cnt, v;
  asm volatile("s_waitcnt lgkmcnt(0)\n\t"
               "v_readfirstlane_b32 %0, %6\n\t"
               "s_cmp_ge_u32 %0, %4\n\t"
               "s_cbranch_scc1 .Lfd%=\n\t"
               "s_mov_b32 %1, 0x20000\n"
               ".Lfw%=:\n\t"
               "s_sleep %5\n\t"
               "ds_read_b32 %2, %3\n\t"
               "s_sub_u32 %1, %1, 1\n\t"
               "s_cbranch_scc1 .Lfe%=\n\t"
               "s_waitcnt lgkmcnt(0)\n\t"
               "v_readfirstlane_b32 %0, %2\n\t"
               "s_cmp_lt_u32 %0, %4\n\t"
               "s_cbranch_scc1 .Lfw%=\n"
               ".Lfe%=:\n\t"
               "s_waitcnt lgkmcnt(0)\n"
               ".Lfd%=:"
               : "=&s"(t), "=&s"(cnt), "=&v"(v) : "v"(addr), "s"(want), "i"(SLEEP), "v"(pre) : "memory", "scc");
}
template <int SLEEP = 1> DEV void flag_wait(u32 addr, u32 want) {
  u32 pre;
  flag_pre(pre, addr);
  flag_wait_pre<SLEEP>(pre, addr, want);
}
DEV void gflag_st(u32x4 srd, u32 off, u32 tag, u32 mask = 1) {
  asm volatile("s_mov_b32 exec_lo, %3\n\ts_mov_b32 exec_hi, 0\n\tbuffer_store_dword %0, off, %1, %2 sc1\n\ts_mov_b64 exec, -1" ::"v"(tag), "s"(srd), "s"(off), "s"(mask) : "memory");
}
DEV void gflag_st_last(u32 addr, u32 last, u32x4 srd, u32 off, u32 tag) {
  u32 old, t;
  u64 m;
  asm volatile("s_mov_b64 exec, 1\n\t"
               "ds_add_rtn_u32 %0, %3, %4\n\t"
               "s_waitcnt lgkmcnt(0)\n\t"
               "v_readfirstlane_b32 %1, %0\n\t"
               "s_cmp_eq_u32 %1, %5\n\t"
               "s_cselect_b64 %2, 1, 0\n\t"
               "s_mov_b64 exec, %2\n\t"
               "buffer_store_dword %8, off, %6, %7 sc1\n\t"
               "s_mov_b64 exec, -1"
               : "=&v"(old), "=&s"(t), "=&s"(m) : "v"(addr), "v"(1u), "s"(last), "s"(srd), "s"(off), "v"(tag) : "memory", "scc");
}
DEV void gflag_wait(u32x4 srd, u32 voff, u32 tag) {
  u32 v, cnt;
  asm volatile("s_mov_b32 %1, 0x4000\n"
               ".Lgw%=:\n\t"
               "buffer_load_dword %0, %2, %3, 0 offen sc1\n\t"
               "s_waitcnt vmcnt(0)\n\t"
               "v_cmp_ne_u32 vcc, %4, %0\n\t"
               "s_cbranch_vccz .Lgd%=\n\t"
               "s_sleep 2\n\t"
               "s_sub_u32 %1, %1, 1\n\t"
               "s_cbranch_scc0 .Lgw%=\n"
               ".Lgd%=:"
               : "=&v"(v), "=&s"(cnt) : "v"(voff), "s"(srd), "s"(tag) : "memory", "scc", "vcc");
}

DEV float gelu_f(float a) {
  const float k0 = -2.3022081999f, k1 = -0.1029432397f;
  const float e = __builtin_amdgcn_exp2f(a * __builtin_fmaf(a * a, k1, k0));
  return a * __builtin_amdgcn_rcpf(1.0f + e);
}
template <int CTRL> DEV float dppf(float x) { return __builtin_bit_cast(float, __builtin_amdgcn_update_dpp(0, __builtin_bit_cast(int, x), CTRL, 0xf, 0xf, false)); }
#define MFMA16(a, b, c) __builtin_amdgcn_mfma_f32_16x16x32_bf16(a, b, c, 0, 0, 0)
#define MFMA32H(a, b, c) __builtin_amdgcn_mfma_f32_32x32x16_f16(a, b, c, 0, 0, 0)

DEV void mfma16_ip(f32x4& c, bf16x8 a, bf16x8 b) { asm volatile("v_mfma_f32_16x16x32_bf16 %0, %1, %2, %0" : "+v"(c) : "v"(a), "v"(b)); }
DEV h2_t hmul(h2_t x, h2_t e) { return __builtin_elementwise_fma(h2_t{x.y, x.y}, h2_t{-e.y, e.x}, h2_t{x.x, x.x} * e); }
DEV h2_t toh(f32x2 v) { return __builtin_convertvector(v, h2_t); }
DEV f32x2 csq(f32x2 e) {
  float t, x, y;
  asm("v_mul_f32 %0, %3, %3\n\tv_add_f32 %1, %2, %2\n\tv_fma_f32 %0, %2, %2, -%0\n\tv_mul_f32 %1, %1, %3" : "=&v"(x), "=&v"(y) : "v"(e.x), "v"(e.y));
  (void)t;
  return f32x2{x, y};
}
DEV f32x2 cexp2(float ar, float ai, float l) {
  const float m = __builtin_amdgcn_exp2f(ar * l), th = __builtin_amdgcn_fractf(ai * l);
  return f32x2{__uint_as_float(opq(__float_as_uint(m * __builtin_amdgcn_cosf(th)))), m * __builtin_amdgcn_sinf(th)};
}

struct TUnit { u32 t[4][4], o[4][4]; };
#define TSTEP_ASM(qp) \
  "s_mov_b32 vcc_lo, %24\n\ts_mov_b32 vcc_hi, %24\n\t" \
  "v_cndmask_b32_dpp %0, %16, %8, vcc quad_perm:" qp " row_mask:0xf bank_mask:0xf\n\t" \
  "v_cndmask_b32_dpp %1, %17, %9, vcc quad_perm:" qp " row_mask:0xf bank_mask:0xf\n\t" \
  "v_cndmask_b32_dpp %2, %18, %10, vcc quad_perm:" qp " row_mask:0xf bank_mask:0xf\n\t" \
  "v_cndmask_b32_dpp %3, %19, %11, vcc quad_perm:" qp " row_mask:0xf bank_mask:0xf\n\t" \
  "v_cndmask_b32_dpp %4, %20, %12, vcc quad_perm:" qp " row_mask:0xf bank_mask:0xf\n\t" \
  "v_cndmask_b32_dpp %5, %21, %13, vcc quad_perm:" qp " row_mask:0xf bank_mask:0xf\n\t" \
  "v_cndmask_b32_dpp %6, %22, %14, vcc quad_perm:" qp " row_mask:0xf bank_mask:0xf\n\t" \
  "v_cndmask_b32_dpp %7, %23, %15, vcc quad_perm:" qp " row_mask:0xf bank_mask:0xf"
#define TSTEP_OPS(o, g0, g1, s, x) \
  : "=&v"(o[g0][0]), "=&v"(o[g0][1]), "=&v"(o[g0][2]), "=&v"(o[g0][3]), "=&v"(o[g1][0]), "=&v"(o[g1][1]), "=&v"(o[g1][2]), "=&v"(o[g1][3]) \
  : "v"(s[g0][0]), "v"(s[g0][1]), "v"(s[g0][2]), "v"(s[g0][3]), "v"(s[g1][0]), "v"(s[g1][1]), "v"(s[g1][2]), "v"(s[g1][3]), \
    "v"(s[(g0) xor (x)][0]), "v"(s[(g0) xor (x)][1]), "v"(s[(g0) xor (x)][2]), "v"(s[(g0) xor (x)][3]), "v"(s[(g1) xor (x)][0]), "v"(s[(g1) xor (x)][1]), "v"(s[(g1) xor (x)][2]), "v"(s[(g1) xor (x)][3])
DEV void tstep(TUnit& U, const u32x4 (&r)[4], int k) {
  if (k == 0) asm volatile(TSTEP_ASM("[1,0,3,2]") TSTEP_OPS(U.t, 0, 2, r, 1), "i"(0x55555555u) : "vcc");
  if (k == 1) asm volatile(TSTEP_ASM("[1,0,3,2]") TSTEP_OPS(U.t, 1, 3, r, 1), "i"(0xAAAAAAAAu) : "vcc");
  if (k == 2) asm volatile(TSTEP_ASM("[2,3,0,1]") TSTEP_OPS(U.o, 0, 1, U.t, 2), "i"(0x33333333u) : "vcc");
  if (k == 3) asm volatile(TSTEP_ASM("[2,3,0,1]") "\n\ts_nop 1" TSTEP_OPS(U.o, 2, 3, U.t, 2), "i"(0xCCCCCCCCu) : "vcc");
}
DEV u32x4 tout(const TUnit& U, int g) { return u32x4{U.o[g][0], U.o[g][1], U.o[g][2], U.o[g][3]}; }

DEV void a_load(u32x4 (&R)[8][4], u32x4 srd, u32 voff, u32 sbase, int n) {
  const int un = n >> 2;
  bload(R[un][n & 3], srd, voff, (sbase ^ (u32)((un >> 2) * 256)) + (u32)(((un & 3) * 16 + (n & 3)) * 4096));
}
DEV void a_take(bf16x8 (&A)[8][4], const TUnit& U, int un) {
#pragma unroll
  for (int g = 0; g < 4; ++g) A[4 * (un >> 2) + g][un & 3] = as8(tout(U, g));
}
template <bool WAIT = true> DEV void a_all(bf16x8 (&A)[8][4], const u32x4 (&R)[8][4]) {
#pragma unroll
  for (int un = 0; un < 8; ++un) {
    if (WAIT) WAITV(28 - 4 * un);
    SCHED();
    TUnit U;
#pragma unroll
    for (int k = 0; k < 4; ++k) tstep(U, R[un], k);
    a_take(A, U, un);
    SCHED();
  }
}

constexpr int WIN = 10, S_H0 = 23, S_H1 = 39, S_WO = 47 - WIN;
template <bool G> constexpr int z_younger(int n) {
  int c = (n + WIN - 1 < 47 ? n + WIN - 1 : 47) - n;
  if (G) c += (S_H0 + WIN >= n && S_H0 < n) + (S_H1 + WIN >= n && S_H1 < n);
  else if (n > S_WO) c += 2 * (n - S_WO);
  return c;
}
template <bool G>
DEV void tail(char* ws, const bf16x8 (&A)[8][4], const u32 (&z1p)[6][2], float bsp, u32 tag, int w, int lane, int ti, int tj TSARG) {
  const __amdgpu_buffer_rsrc_t r_ws = mk_rsrc(ws, WS_END);
  const u32x4 s_ws = mk_srd(ws, WS_END);
  const int c16 = lane & 15, q = lane >> 4;
  __builtin_amdgcn_s_setprio(0);
  STAMP(6);
  const int ksl = (w + ti + (tj & 2) * 2) & 7;
  gflag_wait(s_ws, WS_FZ2 + (u32)((tj * 32 + 4 * ksl + (lane & 3)) * 256), tag);
  STAMP(7);
  const u32 zb = WS_Z2 + (u32)((tj * 6 * 64 + 8 * ksl) * 1024 + lane * 16);
  u32x4 win[WIN];
  const int rot3 = ti % 3;
  const int pact[3] = {rot3, rot3 == 2 ? 0 : rot3 + 1, rot3 == 0 ? 2 : rot3 - 1};
  auto zload = [&](int n) { bload_sc1(win[n % WIN], s_ws, zb, (u32)(pact[n >> 4] * 131072 + ((n >> 3) & 1) * 65536 + ((n & 7) ^ (4 * (tj & 1))) * 1024)); };
  const KP kp = kargs();
  const u32x4 s_wo = mk_srd(kp->w_out, FH * DM * 2);
  u16* const outp = kp->out;
  u32 wov[5];
  auto wov_init = [&]() {
    const int ln = (int)opq(lane);
#pragma unroll
    for (int u = 0; u < 5; ++u) { const int off = u * 1024 + ln * 16; wov[u] = (u32)((off / 160) * 1280 + (off % 160)); }
  };
  auto wo_issue1 = [&](int p, int jj, int u, u32 dst) {
    dma16(s_wo, wov[u], (u32)((96 * jj + 32 * pact[p]) * 1280 + 160 * tj), dst + (u32)(jj * 5120 + u * 1024));
  };
  auto wo_issue = [&](int p, int jj, u32 dst) {
#pragma unroll
    for (int u = 0; u < 5; ++u) dma16(s_wo, wov[u], (u32)((96 * jj + 32 * pact[p]) * 1280 + 160 * tj), dst + (u32)(jj * 5120 + u * 1024));
  };
  const u32 rwb = opq(L_RED + (u32)(w * 4096 + lane * 16));
  const int rt_own = 2 * (w & 1) + ((w >> 1) & 1);
  const u32 rrb = opq(L_RED + (u32)(rt_own * 1024 + lane * 16));
  u32 ho[4];
  f32x4 sum, rv[2];
  auto red_ld = [&](int e, int k) {
    rv[0] = *lp<f32x4>(rrb, (e & 1) * 32768 + 2 * k * 4096);
    rv[1] = *lp<f32x4>(rrb, (e & 1) * 32768 + (2 * k + 1) * 4096);
  };
  auto red_add = [&](int k) { sum = k ? sum + (rv[0] + rv[1]) : rv[0] + rv[1]; asm volatile("" : "+v"(sum)); };
  auto red_fin = [&](int e) {
#pragma unroll
    for (int k = 0; k < 2; ++k)
      ho[2 * (e & 1) + k] = pk2(bf2f(z1p[e][k] & 0xffffu) * (sum[2 * k] + bsp), bfhi(z1p[e][k]) * (sum[2 * k + 1] + bsp));
    if (e & 1)
      __builtin_amdgcn_raw_buffer_store_b128(u32x4{ho[0], ho[1], ho[2], ho[3]}, r_ws,
                                             opq(lane * 16) + WS_H + (u32)((((ti * 4 + rt_own) * 3 + (e >> 1)) * 8 + tj) * 1024), 0, 16);
  };
  const u32 w0mask = __builtin_amdgcn_readfirstlane(w == 0 ? 1 : 0);
#pragma unroll
  for (int n = 0; n < WIN; ++n) zload(n);
  f32x4 acc[2][4];
#pragma unroll
  for (int n = 0; n < 48; ++n) {
    const int e = n >> 3, ks = n & 7;
    WAITV_PIN(z_younger<G>(n), win[n % WIN]);
    const bf16x8 zf = as8(win[n % WIN]);
#pragma unroll
    for (int m = 0; m < 4; ++m) acc[e & 1][m] = MFMA16(zf, A[ks][m], ks ? acc[e & 1][m] : f32x4{});
    SCHED();
    if (n + WIN < 48) zload(n + WIN);
    if (e > 0 && ks < 4) *lp<f32x4>(rwb, ((e - 1) & 1) * 32768 + ks * 1024) = acc[(e - 1) & 1][ks];
    if (G && e > 0 && ks >= 4) { red_add(ks - 4); if (ks < 7) red_ld(e - 1, ks - 3); else red_fin(e - 1); }
    if (ks == 3) {
      if (G && e == 4) WAITV(WIN);
      WAITL0();
      HWBAR();
      if (G && e > 0) red_ld(e - 1, 0);
      if (G && e == 4) gflag_st(s_ws, WS_FH + (u32)((ti * 8 + tj) * 256), tag, w0mask);
    }
    if (n == S_WO - 1) wov_init();
    if (!G && n >= S_WO && n < S_WO + 10) {
#pragma unroll
      for (int k = 2 * (n - S_WO); k < 2 * (n - S_WO) + 2; ++k) wo_issue1(k / 10, 2 * (w - 4) + (k % 10) / 5, k % 5, L_WO + (k / 10) * 40960);
    }
    SCHED();
  }
#pragma unroll
  for (int m = 0; m < 4; ++m) *lp<f32x4>(rwb, 32768 + m * 1024) = acc[1][m];
  if (G) WAITV(0);
  WAITL0();
  HWBAR();
  STAMP(8);
  if (G) {
    {
      f32x4 v[8];
#pragma unroll
      for (int k = 0; k < 8; ++k) v[k] = *lp<f32x4>(rrb, 32768 + k * 4096);
      sum = ((v[0] + v[1]) + (v[2] + v[3])) + ((v[4] + v[5]) + (v[6] + v[7]));
    }
    red_fin(5);
    gflag_st(s_ws, WS_FH + (u32)(((32 + ti) * 8 + tj) * 256), tag, w0mask);
    WAITV(0);
    gflag_st_last(faddr(F_H2CNT), 3, s_ws, WS_FH + (u32)(((2 * 32 + ti) * 8 + tj) * 256), tag);
    STAMP(9);
    flag_wait(faddr(F_H2CNT), 4);
    wo_issue(2, 2 * w, L_RED);
    wo_issue(2, 2 * w + 1, L_RED);
    WAITV(0);
    flag_inc(faddr(F_WO2));
    STAMP(10);
  } else {
    const int rt = w - 4;
    const int a4 = (lane >> 2) & 3, p4 = lane & 3;
    const u32 hb = WS_H + (u32)((ti * 4 + rt) * 3 * 8192 + lane * 16);
    u32x4 hf[2][8];
    auto hload = [&](int p) {
#pragma unroll
      for (int jj = 0; jj < 8; ++jj) hf[p & 1][jj] = __builtin_amdgcn_raw_buffer_load_b128(r_ws, hb, p * 8192 + jj * 1024, 16);
    };
    gflag_wait(s_ws, lane < 32 ? WS_FA + (u32)((tj * 32 + rt * 8 + (lane & 7)) * 256) : WS_FH + (u32)((ti * 8 + (lane & 7)) * 256), tag);
    u32x4 av[3];
    {
      const u32 ao = WS_A + (u32)((tj * 80 + rt * 20 + (lane >> 3)) * 4096 + ti * 128 + (lane & 7) * 16);
      av[0] = __builtin_amdgcn_raw_buffer_load_b128(r_ws, ao, 0, 16);
      av[1] = __builtin_amdgcn_raw_buffer_load_b128(r_ws, ao, 8 * 4096, 16);
      av[2] = __builtin_amdgcn_raw_buffer_load_b128(r_ws, ao - (u32)((lane >> 5) * 4 * 4096), 16 * 4096, 16);
    }
    hload(0);
    STAMP(9);
    {
      const u32 aw = opq(L_AT + (u32)((rt * 20 + (lane >> 3)) * 160 + (lane & 7) * 16));
      *lp<u32x4>(aw) = av[0];
      *lp<u32x4>(aw, 8 * 160) = av[1];
      *lp<u32x4>(aw - (u32)((lane >> 5) * 4 * 160), 16 * 160) = av[2];
    }
    flag_inc(faddr(F_WO01));
    gflag_wait(s_ws, WS_FH + (u32)(((32 + ti) * 8 + (lane & 7)) * 256), tag);
    hload(1);
    flag_wait(faddr(F_WO01), 4);
    f32x4 o[5];
    {
      const float gate = *lp<float>(L_GATE + (u32)((rt * 16 + c16) * 4));
      const u32 ab = opq(L_AT + (u32)((4 * q + a4) * 160 + rt * 32 + 8 * p4));
#pragma unroll
      for (int e = 0; e < 5; ++e) {
        const u32x2 v = __builtin_bit_cast(u32x2, ldstr(ab, 2560 * e));
        o[e] = f32x4{gate * bf2f(v.x & 0xffffu), gate * bfhi(v.x), gate * bf2f(v.y & 0xffffu), gate * bfhi(v.y)};
      }
    }
    flag_inc(faddr(F_AT));
    const u32 wrd = (u32)((4 * q + a4) * 160 + 8 * p4);
#pragma unroll
    for (int p = 0; p < 2; ++p) {
      const u32 wb = opq(wrd + L_WO + p * 40960);
#pragma unroll
      for (int jj = 0; jj < 8; ++jj)
#pragma unroll
        for (int e = 0; e < 5; ++e) {
          const u32 ad = (u32)(jj * 5120 + 32 * e);
          o[e] = MFMA16(cat8(ldstr(wb, ad), ldstr(wb, ad + 2560)), as8(hf[p][jj]), o[e]);
        }
    }
    SCHED();
    flag_wait(faddr(F_WO2), 4);
    bf16x8 wf[8][5];
    {
      const u32 wb = opq(wrd + L_RED);
#pragma unroll
      for (int jj = 0; jj < 8; ++jj)
#pragma unroll
        for (int e = 0; e < 5; ++e) wf[jj][e] = cat8(ldstr(wb, jj * 5120 + 32 * e), ldstr(wb, jj * 5120 + 32 * e + 2560));
    }
    gflag_wait(s_ws, WS_FH + (u32)(((64 + ti) * 8 + (lane & 7)) * 256), tag);
    STAMP(10);
    hload(2);
    gflag_st(s_ws, WS_EPOCH, tag, __builtin_amdgcn_readfirstlane((blockIdx.x == 0 && w == 4) ? 1 : 0));
    flag_wait(faddr(F_AT), 4);
#pragma unroll
    for (int jj = 0; jj < 8; ++jj)
#pragma unroll
      for (int e = 0; e < 5; ++e) o[e] = MFMA16(wf[jj][e], as8(hf[0][jj]), o[e]);
    const u32 at = L_AT + rt * 2560, ob = opq(at + (u32)(c16 * 160 + 8 * q)), atl = opq(at + lane * 16);
#pragma unroll
    for (int e = 0; e < 5; ++e) *lp<u32x2>(ob, 32 * e) = u32x2{pk2(o[e][0], o[e][1]), pk2(o[e][2], o[e][3])};
    CBAR();
    const __amdgpu_buffer_rsrc_t r_out = mk_rsrc(outp, SEQ * DM * 2);
#pragma unroll
    for (int u = 0; u < 3; ++u) {
      if (u < 2 || lane < 32) {
        const u32x4 v = *lp<u32x4>(atl, u * 1024);
        __builtin_amdgcn_raw_buffer_store_b128(v, r_out, wov[u] + (u32)((ti * 64 + rt * 16) * 1280 + 160 * tj), 0, 0);
      }
    }
    STAMP(11);
  }
  TSFLUSH();
}

#define CODE_LINES 440u
template <int V> struct IC { static constexpr int value = V; };
template <int I, int N, class F> DEV void sfor(F&& f) {
  if constexpr (I < N) { f(IC<I>{}); sfor<I + 1, N>(f); }
}
constexpr int c_slot(int c) { return c < 15 ? c % 7 : (c - 12) % 7; }
constexpr int PUB_IT = 15;
constexpr int g_wait(int it) { return it == 10 ? 4 : it == 12 ? 4 : it == 14 ? 3 : it == 16 ? 3 : 6; }
DEV void w_offsets(u32 (&wv)[3], u32 (&wv1)[3], int lane, int ti, int tj, u32 base) {
#pragma unroll
  for (int u = 0; u < 3; ++u) {
    const int off = u * 1024 + lane * 16, row = off / 192, ch = ((off % 192) >> 4) ^ (2 * (row >> 3));
    const int pr3 = (ch >> 2) + ti % 3;
    const int grow = (row & 7) + 32 * (row >> 3);
    wv[u] = base + (u32)(grow * 3072 + ch * 16 - u * 1024 + tj * 192 + FH * 2);
    wv1[u] = base + (u32)(grow * 3072 + (pr3 >= 3 ? pr3 - 3 : pr3) * 64 + (ch & 3) * 16 - u * 1024 + tj * 192);
  }
}
DEV void flag4_pre(u32 (&pre)[4], u32 addr) {
  asm volatile("ds_read2_b32 %0, %2 offset1:1\n\tds_read2_b32 %1, %2 offset0:2 offset1:3" : "=v"(*(u64*)&pre[0]), "=v"(*(u64*)&pre[2]) : "v"(addr) : "memory");
}
#define LSLEEP "2"
#define PUB_ASM(t, u, v, addr, issued) \
  "s_getreg_b32 " t ", hwreg(HW_REG_IB_STS)\n\t" \
  "s_lshr_b32 " u ", " t ", 18\n\t" \
  "s_and_b32 " t ", " t ", 15\n\t" \
  "s_and_b32 " u ", " u ", 48\n\t" \
  "s_or_b32 " t ", " t ", " u "\n\t" \
  "v_sub_u32 " v ", " issued ", " t "\n\t" \
  "ds_write_b32 " addr ", " v "\n\t"
DEV void publish(u32 addr, u32 issued) {
  u32 t, u, v;
  asm volatile(PUB_ASM("%0", "%1", "%2", "%3", "%4") : "=&s"(t), "=&s"(u), "=&v"(v) : "v"(addr), "v"(issued) : "memory", "scc");
}
DEV void flag4_wait(const u32 (&pre)[4], u32 addr, u32 want, u32 faddr_full, u32 issued) {
  u32 t, cnt, u, v0, v1, v2, v3;
  asm volatile("s_waitcnt lgkmcnt(0)\n\t"
               "v_min3_u32 %3, %7, %8, %9\n\t"
               "v_min_u32 %3, %3, %10\n\t"
               "s_mov_b32 %1, 0x20000\n\t"
               "v_readfirstlane_b32 %0, %3\n\t"
               "s_cmp_ge_u32 %0, %12\n\t"
               "s_cbranch_scc1 .Lfd%=\n"
               ".Lfw%=:\n\t"
               PUB_ASM("%0", "%2", "%3", "%13", "%14")
               "s_sleep " LSLEEP "\n\t"
               "ds_read_b32 %3, %11\n\t"
               "ds_read_b32 %4, %11 offset:4\n\t"
               "ds_read_b32 %5, %11 offset:8\n\t"
               "ds_read_b32 %6, %11 offset:12\n\t"
               "s_waitcnt lgkmcnt(0)\n\t"
               "v_min3_u32 %3, %3, %4, %5\n\t"
               "v_min_u32 %3, %3, %6\n\t"
               "s_sub_u32 %1, %1, 1\n\t"
               "s_cbranch_scc1 .Lfd%=\n\t"
               "v_readfirstlane_b32 %0, %3\n\t"
               "s_cmp_lt_u32 %0, %12\n\t"
               "s_cbranch_scc1 .Lfw%=\n"
               ".Lfd%=:"
               : "=&s"(t), "=&s"(cnt), "=&s"(u), "=&v"(v0), "=&v"(v1), "=&v"(v2), "=&v"(v3)
               : "v"(pre[0]), "v"(pre[1]), "v"(pre[2]), "v"(pre[3]), "v"(addr), "i"(want), "v"(faddr_full), "v"(issued) : "memory", "scc");
}

__global__ __launch_bounds__(512, 2) void fused_kernel(Params P_unused) {
  extern __shared__ __attribute__((aligned(16))) char smem[];
  const int lane = threadIdx.x & 63;
  const int w = __builtin_amdgcn_readfirstlane(threadIdx.x >> 6);
  const int b = blockIdx.x, xcd = b & 7, idx = b >> 3;
  u32 pf_raw;
  {
    u64 pc;
    asm volatile("s_getpc_b64 %0" : "=s"(pc));
    const u32 ln = (u32)(((idx & 3) * 8 + w) * 16 + (lane & 15));
    asm volatile("global_load_dword %0, %1, %2" : "=v"(pf_raw) : "v"((ln < CODE_LINES ? ln : 0u) * 128u), "s"(pc) : "memory");
  }
  const int ti = (xcd >> 1) * 8 + (idx >> 2), tj = (xcd & 1) * 4 + (idx & 3);
  const int c16 = lane & 15, q = lane >> 4, a4 = (lane >> 2) & 3, p4 = lane & 3;
  char* ws = kargs()->ws;
  const __amdgpu_buffer_rsrc_t r_ws = mk_rsrc(ws, WS_END);
  const u32 a_v = (u32)(4 * a4 * 4096 + (4 * q + p4) * 16);
  const u32 a_sb = (u32)(ti * 64 * 4096 + ((w + ti + (tj & 2) * 2) & 7) * 512 + (tj & 1) * 256);

  STAMP(0);
  if (w == 4) *lp<u32>(L_FLG + 4 * lane) = 0;
  const u32x4 s_ws = mk_srd(ws, WS_END);
  const int kh = (w >> 1) & 1, mh = w & 1, rt_own = 2 * mh + kh;
  u32 ep_raw, bsp_raw;
  asm volatile("buffer_load_dword %0, off, %1, 0 sc1" : "=v"(ep_raw) : "s"(s_ws) : "memory");

#define INITBAR() do { WAITL0(); HWBAR(); } while (0)
  const int rot5 = (ti + tj) % 5;
  u32 lin[5];
#pragma unroll
  for (int l = 0; l < 5; ++l) { const int r = l + rot5; lin[l] = (u32)(r >= 5 ? r - 5 : r); }
  u32x4 s_wsp;
  u32x4 R[8][4];
  bf16x8 A[8][4];
  u32 z1p[6][2] = {};
  float bsp = 0.f;
  u32 tag;

  if (w < 4) {
    __builtin_amdgcn_s_setprio(GPRIO);
    const KP kp = kargs();
    const u32x4 s_x = mk_srd(kp->u, SEQ * DM * 2), s_w = mk_srd(kp->w_in, DM * FF * 2);
    const u32 x_v = (u32)((4 * a4 + 16 * ((q >> 1) ^ kh)) * 1280 + (4 * (q & 1) + p4) * 16 + kh * 128);
    u32x4 XR[5][4];
    asm volatile("buffer_load_ushort %0, %1, %2, 0 offen" : "=v"(bsp_raw) : "v"((u32)((ti * 64 + rt_own * 16 + c16) * 2)), "s"(mk_srd(kp->b_sp, SEQ * 2)) : "memory");
#pragma unroll
    for (int k = 0; k < 20; ++k) {
      bload(XR[k >> 2][k & 3], s_x, x_v, (u32)((ti * 64 + 32 * mh + (k & 3)) * 1280) + lin[k >> 2] * 256);
      if (k == 3) INITBAR();
    }
    u32 wv[3], wv1[3];
    w_offsets(wv, wv1, lane, ti, tj, (u32)((64 * kh + 8 * mh) * 3072));
    auto w_issue = [&](int c, int u) {
      const u32 so = lin[(c % 10) >> 1] * (128 * 3072), vo = c < 10 ? wv[u] : wv1[u];
      if (u == 0) { m0_set(L_RING + c_slot(c) * CHUNK + w * 3072); asm volatile("s_nop 0"); dma16o<0>(s_w, vo, so); }
      else if (u == 1) dma16o<1024>(s_w, vo, so);
      else dma16o<2048>(s_w, vo, so);
    };
    bf16x8 X[10][2];
#pragma unroll
    for (int l = 0; l < 5; ++l) {
      if (l < 4) w_issue(2 * l, 0);
      if (l == 0) {
        asm volatile("s_waitcnt vmcnt(17)\n\tv_readfirstlane_b32 %0, %2\n\tv_lshlrev_b32 %1, 16, %3\n\ts_nop 1"
                     : "=s"(tag), "=v"(bsp) : "v"(ep_raw), "v"(bsp_raw), "v"(pf_raw) : "memory");
        tag += 1u;
      } else {
        WAITV(l < 4 ? 17 - l : 12);
      }
      SCHED();
      TUnit U;
#pragma unroll
      for (int k = 0; k < 4; ++k) {
        tstep(U, XR[l], k);
        if (l < 4 && k == 0) w_issue(2 * l, 1);
        if (l < 4 && k == 2) w_issue(2 * l, 2);
      }
#pragma unroll
      for (int sq = 0; sq < 2; ++sq) {
        u32x4 o0, o1;
#pragma unroll
        for (int d = 0; d < 4; ++d) { const auto r = __builtin_amdgcn_permlane32_swap(U.o[2 * sq][d], U.o[2 * sq + 1][d], false, false); o0[d] = r[0]; o1[d] = r[1]; }
        X[2 * l + sq][0] = as8(o0);
        X[2 * l + sq][1] = as8(o1);
      }
      SCHED();
    }
    const u32 wb = L_RING + (u32)(kh * 6144 + (8 * q + a4) * 192 + (p4 >> 1) * 16 + (p4 & 1) * 8);
    const u32 wbe = opq(wb + 32 * (q & 1)), wbo = opq(wb + 32 * (1 - (q & 1)));
    const u32 wbe2 = opq(wbe + 4 * CHUNK), wbo2 = opq(wbo + 4 * CHUNK), gwb = L_GW + (u32)(kh * 128 + (4 * (q & 1) + (q >> 1)) * 16);
    s16x4 Wh[2][6][2];
    auto w_half = [&](int n, int k) {
      const int sl = c_slot(n), e = k >> 1;
      const u32 ad = (u32)((sl & 3) * CHUNK + 64 * (e >> 1) + 768 * (k & 1)), bs = sl < 4 ? ((e & 1) ? wbo : wbe) : ((e & 1) ? wbo2 : wbe2);
      Wh[n & 1][e][k & 1] = ldstr(bs, ad);
    };
    auto Wf = [&](int n, int e) { return cat8(Wh[n & 1][e][0], Wh[n & 1][e][1]); };
    const u32 f_me = opq(faddr(F_CH) + 4 * w), f_pa = opq(faddr(F_CH) + 4 * (w ^ 1)), f_ex = opq(faddr(F_CH) + 4 * (w ^ 2)), f_full = opq(faddr(F_FULL));
    WAITV(9);
    flag_st(f_me, 1);
    STAMP(1);
    flag_wait(f_pa, 1);
#pragma unroll
    for (int k = 0; k < 12; ++k) w_half(0, k);
    flag_st(f_me, 2);
    const u32 fb = (u32)(10 + 6 * kh);
    u32 pre;
    flag_pre(pre, f_full);
    auto step_wait = [&](int it) {
      if (it & 1) flag_wait_pre(pre, f_pa, it + 2); else flag_wait_pre(pre, f_full, fb + 6 * it);
      __builtin_amdgcn_s_waitcnt(0xC07F);
      SCHED();
    };
    auto step_post = [&](int it) {
      if (!(it & 1)) WAITV(g_wait(it));
      flag_st(f_me, it + 3);
    };
    const u32 exw = opq(L_RING + (u32)((1 + mh) * CHUNK + kh * 6144 + lane * 16)), exr = opq(L_RING + (u32)((1 + mh) * CHUNK + (kh ^ 1) * 6144 + lane * 16));
    f32x4 acc2[6], acc1[6], accg;
    {
      f32x4 ac[2][6] = {};
#pragma unroll
      for (int it = 0; it < 10; ++it) {
        step_wait(it);
#pragma unroll
        for (int k = 0; k < 12; ++k) {
          const int m = k < 6, e = k % 6;
          ac[m][e] = MFMA16(X[it][m], Wf(it, e), ac[m][e]);
          SCHED();
          if (k < 8) { w_half(it + 1, k + (k >> 1)); if (k & 1) w_half(it + 1, k + (k >> 1) + 1); }
          if (k == 7) step_post(it);
          if (k == 8) flag_pre(pre, (it & 1) ? f_full : f_pa);
          if ((it & 1) && it < 8 && k >= 9) w_issue(it + 7, k - 9);
          if (it == 9 && k >= 6) *lp<f32x4>(exw, e * 1024) = ac[1][e];
          SCHED();
        }
      }
      flag_st(f_me + 16, 1);
      flag_wait(f_ex + 16, 1);
#pragma unroll
      for (int e = 0; e < 6; ++e) acc2[e] = ac[0][e] + *lp<f32x4>(exr, e * 1024);
    }
    STAMP(2);
    {
      float s1[4] = {}, s2[4] = {};
#pragma unroll
      for (int e = 0; e < 6; ++e)
#pragma unroll
        for (int r = 0; r < 4; ++r) { const float g = gelu_f(acc2[e][r]); acc2[e][r] = g; s1[r] += g; s2[r] = __builtin_fmaf(g, g, s2[r]); }
#pragma unroll
      for (int r = 0; r < 4; ++r) {
        s1[r] += dppf<0x128>(s1[r]); s2[r] += dppf<0x128>(s2[r]);
        s1[r] += dppf<0x124>(s1[r]); s2[r] += dppf<0x124>(s2[r]);
        s1[r] += dppf<0x122>(s1[r]); s2[r] += dppf<0x122>(s2[r]);
        s1[r] += dppf<0x121>(s1[r]); s2[r] += dppf<0x121>(s2[r]);
      }
      const float a1 = (p4 & 2) ? ((p4 & 1) ? s1[3] : s1[2]) : ((p4 & 1) ? s1[1] : s1[0]);
      const float a2 = (p4 & 2) ? ((p4 & 1) ? s2[3] : s2[2]) : ((p4 & 1) ? s2[1] : s2[0]);
      __builtin_amdgcn_raw_buffer_store_b128(u32x4{__float_as_uint(a1), tag, __float_as_uint(a2), tag}, r_ws,
                                             c16 < 4 ? WS_STATS + (u32)(((ti * 4 + rt_own) * 8 + tj) * 256 + (4 * q + c16) * 16) : WS_END, 0, 16);
    }
    STAMP(3);
    auto publish_z2 = [&]() {
      const u32 so = WS_STATS + (u32)((ti * 4 + rt_own) * 2048 + (c16 >> 2) * 256 + (4 * q + p4) * 16);
      u32x4 g0, g1;
#pragma nounroll
      for (u32 sp = 0; sp < 0x4000u; ++sp) {
        CBAR();
        g0 = __builtin_amdgcn_raw_buffer_load_b128(r_ws, so, 0, 16);
        g1 = __builtin_amdgcn_raw_buffer_load_b128(r_ws, so + 1024, 0, 16);
        if (__all(g0.y == tag && g0.w == tag && g1.y == tag && g1.w == tag)) break;
        __builtin_amdgcn_s_sleep(2);
      }
      CBAR();
      STAMP(4);
      float t1 = __uint_as_float(g0.x) + __uint_as_float(g1.x), t2 = __uint_as_float(g0.z) + __uint_as_float(g1.z);
      t1 += dppf<0x128>(t1); t2 += dppf<0x128>(t2);
      t1 += dppf<0x124>(t1); t2 += dppf<0x124>(t2);
      const float mean = t1 * (1.0f / FH);
      const float var = fmaxf(__builtin_fmaf(-mean, mean, t2 * (1.0f / FH)), 0.f);
      const float rs = __builtin_amdgcn_rsqf(var + 1e-5f), mo = -mean * rs;
      const float rr[4] = {dppf<0x00>(rs), dppf<0x55>(rs), dppf<0xAA>(rs), dppf<0xFF>(rs)};
      const float mm[4] = {dppf<0x00>(mo), dppf<0x55>(mo), dppf<0xAA>(mo), dppf<0xFF>(mo)};
      const u32 zo = WS_Z2 + (u32)(((tj * 6 + (q & 1)) * 64 + 4 * (ti >> 1) + 2 * kh + (q >> 1)) * 1024 + (c16 + 16 * (2 * (ti & 1) + mh)) * 16);
#pragma unroll
      for (int p = 0; p < 3; ++p) {
        u32 x[2], y[2];
#pragma unroll
        for (int k = 0; k < 2; ++k) {
          x[k] = pk2(__builtin_fmaf(acc2[2 * p][2 * k], rr[2 * k], mm[2 * k]), __builtin_fmaf(acc2[2 * p][2 * k + 1], rr[2 * k + 1], mm[2 * k + 1]));
          y[k] = pk2(__builtin_fmaf(acc2[2 * p + 1][2 * k], rr[2 * k], mm[2 * k]), __builtin_fmaf(acc2[2 * p + 1][2 * k + 1], rr[2 * k + 1], mm[2 * k + 1]));
        }
        const auto s0 = __builtin_amdgcn_permlane16_swap(x[0], y[0], false, false);
        const auto s1 = __builtin_amdgcn_permlane16_swap(x[1], y[1], false, false);
        __builtin_amdgcn_raw_buffer_store_b128(u32x4{s0[0], s1[0], s0[1], s1[1]}, r_ws, zo + p * 131072, 0, 16);
      }
    };
    {
      f32x4 ac[2][6] = {}, ag[2] = {};
#pragma unroll
      for (int it = 10; it < 20; ++it) {
        const bf16x8 gf = lds128(gwb + lin[(it - 10) >> 1] * 256, (it & 1) * 32);
        if (it < 19) step_wait(it);
        else { flag_wait_pre(pre, f_ex, 13); __builtin_amdgcn_s_waitcnt(0xC07F); SCHED(); }
        if (it == PUB_IT) { publish_z2(); SCHED(); }
        if (it == PUB_IT + 3) {
          WAITV(0);
          gflag_st_last(faddr(F_Z2CNT), 3, s_ws, WS_FZ2 + (u32)((tj * 32 + ti) * 256), tag);
          STAMP(5);
          s_wsp = mk_srd(kargs()->w_sp, SEQ * SEQ * 2);
          SCHED();
        }
#pragma unroll
        for (int k = 0; k < 14; ++k) {
          const int m = k < 7, e = k % 7;
          if (e < 6) ac[m][e] = MFMA16(Wf(it, e), X[it - 10][m], ac[m][e]);
          else ag[m] = MFMA16(gf, X[it - 10][m], ag[m]);
          SCHED();
          if (it < 19 && k < 8) { w_half(it + 1, k + (k >> 1)); if (k & 1) w_half(it + 1, k + (k >> 1) + 1); }
          if (k == 7 && it < 18) step_post(it);
          if (k == 9 && it < 19) flag_pre(pre, it == 18 ? f_ex : (it & 1) ? f_full : f_pa);
          if ((it == 11 || it == 13) && k >= 11) w_issue(it + 5, k - 11);
          if (it >= PUB_IT + 3 && (k & 1) && k < 12) a_load(R, s_wsp, a_v, a_sb, 6 * (it - PUB_IT - 3) + (k >> 1));
          if (it == 19 && k >= 7 && e < 6) *lp<f32x4>(exw, e * 1024) = ac[1][e];
          SCHED();
        }
      }
      *lp<f32x4>(L_GX + w * 1024 + lane * 16) = ag[1];
      flag_st(f_me + 16, 2);
#pragma unroll
      for (int e = 0; e < 6; ++e) acc1[e] = ac[0][e];
      accg = ag[0];
    }
    {
      flag_wait(f_ex + 16, 2);
#pragma unroll
      for (int e = 0; e < 6; ++e) acc1[e] += *lp<f32x4>(exr, e * 1024);
      accg += *lp<f32x4>(L_GX + (w ^ 2) * 1024 + lane * 16);
      const float gate = __builtin_amdgcn_rcpf(1.0f + __builtin_amdgcn_exp2f(accg[0] * -1.4426950409f));
      *lp<float>(L_GATE + (u32)((rt_own * 16 + c16) * 4)) = gate;
      const float og = 1.0f - gate;
#pragma unroll
      for (int e = 0; e < 6; ++e) {
        if (e < 5) { a_load(R, s_wsp, a_v, a_sb, 12 + 4 * e); a_load(R, s_wsp, a_v, a_sb, 13 + 4 * e); }
        z1p[e][0] = opq(pk2(gelu_f(acc1[e][0]) * og, gelu_f(acc1[e][1]) * og));
        SCHED();
        if (e < 5) { a_load(R, s_wsp, a_v, a_sb, 14 + 4 * e); a_load(R, s_wsp, a_v, a_sb, 15 + 4 * e); }
        z1p[e][1] = opq(pk2(gelu_f(acc1[e][2]) * og, gelu_f(acc1[e][3]) * og));
        SCHED();
      }
    }
    STAMP(11);
    a_all(A, R);
    tail<true>(ws, A, z1p, bsp, tag, w, lane, ti, tj TSPASS);
    return;
  } else if (w == 7) {
    __builtin_amdgcn_s_setprio(LPRIO);
    const KP kp = kargs();
    const u32x4 s_w = mk_srd(kp->w_in, DM * FF * 2), s_gw = mk_srd(kp->gate_w, DM * 2);
    dma16(s_gw, lane * 16, 0, L_GW);
    dma16(s_gw, lane * 16, 256, L_GW + 256);
    INITBAR();
    u32 wv[3], wv1[3];
    w_offsets(wv, wv1, lane, ti, tj, 16 * 3072);
    const u32 f_cons = opq(faddr(F_CH)), f_full = opq(faddr(F_FULL));
    u32 pre4[4] __attribute__((aligned(8)));
    sfor<0, 10>([&](auto IC_) {
      constexpr int k = decltype(IC_)::value, c = 2 * k + 1;
      if (c >= 7) flag4_wait(pre4, f_cons, c < 15 ? c - 5 : c - 3, f_full, 4 + 12 * k);
#pragma unroll
      for (int qq = 0; qq < 4; ++qq) {
        m0_set(L_RING + c_slot(c) * CHUNK + qq * 3072);
        u32 so = lin[k % 5] * (128 * 3072) + (u32)((64 * (qq >> 1) + 8 * (qq & 1)) * 3072);
        asm volatile("s_nop 0" : "+s"(so));
        dma16o<0>(s_w, c < 10 ? wv[0] : wv1[0], so);
        dma16o<1024>(s_w, c < 10 ? wv[1] : wv1[1], so);
        dma16o<2048>(s_w, c < 10 ? wv[2] : wv1[2], so);
        if ((qq & 1) && k >= 1) publish(f_full, 4 + 12 * k + 3 * qq + 3);
        if (qq == 2 && c >= 5 && c < 19) flag4_pre(pre4, f_cons);
      }
      if (c == 9) flag_st(faddr(F_GO), 1);
      if (c == 5) STAMP(1);
    });
#pragma unroll
    for (int k = 3; k >= 1; --k) { WAITV(6 * k); publish(f_full, 124); }
    asm volatile("s_waitcnt vmcnt(0)\n\tv_readfirstlane_b32 %0, %1\n\ts_nop 1" : "=s"(tag) : "v"(ep_raw), "v"(pf_raw) : "memory");
    tag += 1u;
    flag_st(f_full, 124);
    STAMP(2);
  } else if (w == 6 && !(ti & 1)) {
    __builtin_amdgcn_s_setprio(3);
    const u32x4 s_x = mk_srd(kargs()->u, SEQ * DM * 2);
    const u32 sc = L_SR + 2 * SR_SZ;
#pragma unroll
    for (int u = 0; u < 5; ++u) {
      const int off = u * 1024 + lane * 16;
      const u32 vo = (u32)((off / 160) * 1280 + (off % 160) + ti * 64 * 1280 + tj * 160);
#pragma unroll
      for (int g = 0; g < 4; ++g) dma16(s_x, vo, g * 32 * 1280, sc + g * 5120 + u * 1024);
      if (u == 0) INITBAR();
    }
    asm volatile("s_waitcnt vmcnt(0)\n\tv_readfirstlane_b32 %0, %1\n\ts_nop 1" : "=s"(tag) : "v"(ep_raw), "v"(pf_raw) : "memory");
    tag += 1u;
    const u32 hr = opq(sc + (u32)((8 * q + 3 - a4) * 160 + 8 * p4));
    bf16x8 tv[4][5];
#pragma unroll
    for (int g = 0; g < 4; ++g)
#pragma unroll
      for (int e = 0; e < 5; ++e) tv[g][e] = cat8(ldstr(hr, g * 5120 + 640 + 32 * e), ldstr(hr, g * 5120 + 32 * e));
    CBAR();
#pragma unroll
    for (int g = 0; g < 4; ++g) {
      const u32 tw = opq(sc + (u32)(c16 * 256 + (((15 - 4 * g - q) ^ c16) * 16)));
#pragma unroll
      for (int e = 0; e < 5; ++e) *lp<bf16x8>(tw, e * 4096) = tv[g][e];
    }
    CBAR();
    const u32 uo = WS_UT + (u32)((tj * 80 + q) * 4096 + (240 - 8 * ti + c16) * 16);
#pragma unroll
    for (int k = 0; k < 4; ++k) {
      const u32 tr = opq(sc + (u32)((4 * k + q) * 256 + ((c16 ^ (4 * k + q)) * 16)));
#pragma unroll
      for (int e = 0; e < 5; ++e)
        __builtin_amdgcn_raw_buffer_store_b128(*lp<u32x4>(tr, e * 4096), r_ws, uo + (u32)((16 * e + 4 * k) * 4096), 0, 16);
    }
    STAMP(1);
    WAITV(0);
    gflag_st(s_ws, WS_FUT + (u32)((tj * 32 + ti) * 256), tag);
    STAMP(2);
  } else {
    const int sv = w - 4;
    const int c0 = 80 * tj + (5 * ti) / 2, nch = 2 + (ti & 1);
    constexpr bool has = true;
    const int chn = c0 + sv;
    const u32 sr = L_SR + sv * SR_SZ;
    __builtin_amdgcn_s_setprio(3);
    const KP kp = kargs();
    u32x4 praw[3];
    {
      const int l24 = lane < 24 ? lane : (lane < 48 ? lane - 24 : 0);
      const size_t po = (size_t)chn * NST + l24 * 8;
      praw[0] = *(const u32x4*)((lane < 24 ? kp->Are : kp->Aim) + po);
      praw[1] = *(const u32x4*)((lane < 24 ? kp->Bre : kp->Bim) + po);
      praw[2] = *(const u32x4*)((lane < 24 ? kp->Cre : kp->Cim) + po);
    }
    const float ldt = bf2f(kp->logdt[chn]), dsk = bf2f(kp->dskip[chn]);
    __builtin_amdgcn_s_setprio(SPRIO);
    INITBAR();
    STAMP(1);
    f32x16 kacc = {};
    float kinv = 0.f;
    if (has) {
      const float dt = bf2f(pk2(expf(ldt), 0.f) & 0xffffu);
      const u32 rdp = opq(sr + (u32)((lane & 31) * 272 + (lane >> 5) * 16));
#pragma unroll
      for (int k = 0; k < 3; ++k) *lp<u32x4>(sr + SR_T + 1024 + lane * 16, k * 1024) = praw[k];
      CBAR();
      const u32 prd = opq(sr + SR_T + 1024 + lane * 2);
      m0_set(sr);
      auto putw = [&](int row, u32 v) { asm volatile("ds_write_addtid_b32 %0 offset:%1" ::"v"(v), "i"(row * 272) : "memory"); };
      auto put = [&](int row, h2_t v) { putw(row, __builtin_bit_cast(u32, v)); };
      float ar[3], ai[3], wr[3], wi[3], mx = 0.f;
#pragma unroll
      for (int ps = 0; ps < 3; ++ps) {
        u32 pw[6];
#pragma unroll
        for (int k = 0; k < 6; ++k) pw[k] = *lp<u16>(prd, (k >> 1) * 1024 + (k & 1) * 384 + ps * 128);
        ar[ps] = (dt * bf2f(pw[0])) * 1.4426950409f;
        ai[ps] = (dt * bf2f(pw[1])) * 0.15915494309f;
        const float br = dt * bf2f(pw[2]), bi = dt * bf2f(pw[3]), cr = bf2f(pw[4]), ci = bf2f(pw[5]);
        wr[ps] = __uint_as_float(opq(__float_as_uint(__builtin_fmaf(cr, br, -ci * bi))));
        wi[ps] = -__builtin_fmaf(cr, bi, ci * br);
        mx = fmaxf(mx, fmaxf(fabsf(wr[ps]), fabsf(wi[ps])));
      }
      mx = fmaxf(mx, dppf<0x128>(mx)); mx = fmaxf(mx, dppf<0x124>(mx)); mx = fmaxf(mx, dppf<0x122>(mx)); mx = fmaxf(mx, dppf<0x121>(mx));
      const u32 mb = __float_as_uint(mx);
      const u32 ex = min(max(max(max((u32)__builtin_amdgcn_readlane(mb, 0), (u32)__builtin_amdgcn_readlane(mb, 16)),
                                 max((u32)__builtin_amdgcn_readlane(mb, 32), (u32)__builtin_amdgcn_readlane(mb, 48))) >> 23, 1u), 252u);
      const float wsc = __uint_as_float((253u - ex) << 23);
      kinv = __uint_as_float((ex + 1u) << 23);
      h2_t c1, c2, c4, c256, c512, c1024, g0, qv[32];
      auto setup = [&](int ps) {
        const f32x2 e1 = cexp2(ar[ps], -ai[ps], 1.f), e8 = cexp2(ar[ps], ai[ps], 8.f), e256 = cexp2(ar[ps], -ai[ps], 256.f);
        const f32x2 e2 = csq(e1), e16 = csq(e8), e512 = csq(e256), e4 = csq(e2), e32 = csq(e16), e1024 = csq(e512), e64 = csq(e32), e128 = csq(e64);
        c1 = toh(e1); c2 = toh(e2); c4 = toh(e4); c256 = toh(e256); c512 = toh(e512); c1024 = toh(e1024);
        qv[1] = toh(e8); qv[2] = toh(e16); qv[4] = toh(e32); qv[8] = toh(e64); qv[16] = toh(e128);
        g0 = toh(f32x2{__uint_as_float(opq(__float_as_uint(wr[ps] * wsc))), wi[ps] * wsc});
      };
      qv[0] = h2_t{(_Float16)1.f, (_Float16)0.f};
      setup(0);
#pragma unroll
      for (int ps = 0; ps < 3; ++ps) {
        {
          const h2_t p1 = hmul(g0, c1), p2 = hmul(g0, c2), p4 = hmul(g0, c4), g1 = hmul(g0, c256), g2 = hmul(g0, c512), g4 = hmul(g0, c1024);
          const h2_t p3 = hmul(p1, c2), p258 = hmul(g1, c2), g3 = hmul(g1, c512), p5 = hmul(p1, c4);
          SCHED();
          put(0, g0); put(1, p1); put(2, p2); put(4, p4); put(8, g1); put(12, g2); put(16, g4);
          SCHED();
          const h2_t p6 = hmul(p2, c4), p260 = hmul(g1, c4), p516 = hmul(g2, c4), g5 = hmul(g1, c1024), g6 = hmul(g2, c1024), g8 = hmul(g4, c1024);
          const h2_t p7 = hmul(p3, c4), p262 = hmul(p258, c4), p772 = hmul(g3, c4), g7 = hmul(g3, c1024);
          SCHED();
          put(3, p3); put(9, p258); put(14, g3); put(5, p5); put(6, p6); put(10, p260); put(13, p516);
          SCHED();
          qv[3] = hmul(qv[1], qv[2]); qv[5] = hmul(qv[1], qv[4]); qv[6] = hmul(qv[2], qv[4]); qv[24] = hmul(qv[8], qv[16]);
          qv[9] = hmul(qv[1], qv[8]); qv[10] = hmul(qv[2], qv[8]); qv[12] = hmul(qv[4], qv[8]); qv[7] = hmul(qv[3], qv[4]);
          SCHED();
          put(17, g5); put(18, g6); put(20, g8); put(7, p7); put(11, p262); put(15, p772); put(19, g7);
        }
        h8_t pf[8], qf[8];
#pragma unroll
        for (int kk = 0; kk < 8; ++kk) pf[kk] = *lp<h8_t>(rdp, kk * 32);
        CBAR();
        SCHED();
        qv[11] = hmul(qv[3], qv[8]); qv[13] = hmul(qv[5], qv[8]); qv[14] = hmul(qv[6], qv[8]); qv[15] = hmul(qv[7], qv[8]);
        SCHED();
#pragma unroll
        for (int j = 0; j < 8; ++j) put(j, qv[j]);
        SCHED();
#pragma unroll
        for (int j = 1; j < 8; ++j) qv[16 + j] = hmul(qv[j], qv[16]);
        SCHED();
#pragma unroll
        for (int j = 8; j < 16; ++j) put(j, qv[j]);
        SCHED();
#pragma unroll
        for (int j = 1; j < 8; ++j) qv[24 + j] = hmul(qv[j], qv[24]);
        SCHED();
#pragma unroll
        for (int j = 16; j < 32; ++j) put(j, qv[j]);
#pragma unroll
        for (int kk = 0; kk < 8; ++kk) qf[kk] = *lp<h8_t>(rdp, kk * 32);
        CBAR();
        SCHED();
        if (ps < 2) setup(ps + 1);
#pragma unroll
        for (int kk = 0; kk < 8; ++kk) kacc = MFMA32H(pf[kk], qf[kk], kacc);
        if (ps < 2) {
          __builtin_amdgcn_sched_group_barrier(0x402, 20, 0);
#pragma unroll
          for (int kk = 0; kk < 8; ++kk) { __builtin_amdgcn_sched_group_barrier(0x8, 1, 0); __builtin_amdgcn_sched_group_barrier(0x402, 5, 0); }
        }
        SCHED();
      }
    }
    STAMP(2);
    asm volatile("s_waitcnt vmcnt(0)\n\tv_readfirstlane_b32 %0, %1\n\ts_nop 1" : "=s"(tag) : "v"(ep_raw), "v"(pf_raw) : "memory");
    tag += 1u;
    if (has) {
      const u32 T = sr + SR_T, CB = sr;
      const int bq = lane & 31, hh = lane >> 5;
#pragma unroll
      for (int r = 0; r < 12; ++r) kacc[r] = __uint_as_float(opq(__float_as_uint(kacc[r] * kinv)));
      if (lane == 0) kacc[0] += dsk;
      *lp<u32x2>(T + lane * 8) = u32x2{0u, 0u};
      *lp<u32x2>(T + 512 + (u32)(bq * 16 + hh * 8)) = u32x2{pk2(kacc[0], kacc[1]), pk2(kacc[2], kacc[3])};
      *lp<u32>(CB + (u32)(hh ? 256 + 4 * bq : 8 * bq)) = pk2(kacc[4], kacc[5]);
      *lp<u32>(CB + (u32)(hh ? 384 + 4 * bq : 8 * bq + 4)) = pk2(kacc[6], kacc[7]);
#pragma unroll
      for (int r = 0; r < 4; ++r) *lp<u16>(CB + (u32)(512 + 64 * (4 * hh + r) + 2 * bq)) = (u16)pk2(kacc[8 + r], 0.f);
      CBAR();
      {
        const u32x2 v = *lp<u32x2>(CB + (u32)(8 * bq));
        const u32 n4 = *lp<u16>(CB + (u32)(8 * bq + 8));
        const u32x4 o = {__builtin_amdgcn_perm(v.x, v.x, 0x01000100u), __builtin_amdgcn_perm(v.y, v.x, 0x05040302u),
                         __builtin_amdgcn_perm(v.y, v.y, 0x01000100u), __builtin_amdgcn_perm(n4, v.y, 0x05040302u)};
        *lp<u32x4>(T + 1024 + (u32)(bq * 16)) = o;
      }
      {
        const u32 v = *lp<u32>(CB + (u32)(256 + 4 * lane));
        const u32 n2 = *lp<u16>(CB + (u32)(256 + 4 * lane + 4));
        *lp<u32x4>(T + 1536 + (u32)(lane * 16)) = u32x4{__builtin_amdgcn_perm(v, v, 0x01000100u), v, __builtin_amdgcn_perm(v, v, 0x03020302u), n2 * 0x10001u};
      }
#pragma unroll
      for (int g = 0; g < 2; ++g) {
        const u32 v0 = *lp<u16>(CB + (u32)(512 + 2 * (g * 64 + lane))), v1 = *lp<u16>(CB + (u32)(514 + 2 * (g * 64 + lane)));
        *lp<u32x4>(T + 2560 + (u32)((g * 64 + lane) * 16)) = u32x4{v0 * 0x10001u, v0 * 0x10001u, ((lane & 1) ? v1 : v0) | (v1 << 16), v1 * 0x10001u};
      }
      *lp<u32>(sr + (u32)(q * CS + 4096 + c16 * 4)) = 0u;
      STAMP(5);
      gflag_wait(s_ws, WS_FUT + (u32)((tj * 32 + 2 * c16) * 256), tag);
      const u32 srl = opq(sr + lane * 16);
      u32x4 ur[4];
#pragma unroll
      for (int g = 0; g < 4; ++g) ur[g] = __builtin_amdgcn_raw_buffer_load_b128(r_ws, WS_UT + (u32)(chn * 4096 + lane * 16), g * 1024, 16);
#pragma unroll
      for (int g = 0; g < 4; ++g) *lp<u32x4>(srl, 3 * CS + g * 1024) = ur[g];
      CBAR();
#pragma unroll
      for (int g = 0; g < 4; ++g) {
        const u32 o = (u32)(g * 1024);
        const u32x4 d = ur[g];
        const u32x2 n = *lp<u32x2>(srl, 3 * CS + o + 16);
        const u32 a0 = __builtin_amdgcn_alignbit(d.y, d.x, 16), a1 = __builtin_amdgcn_alignbit(d.z, d.y, 16), a2 = __builtin_amdgcn_alignbit(d.w, d.z, 16),
                  a3 = __builtin_amdgcn_alignbit(n.x, d.w, 16), a4_ = __builtin_amdgcn_alignbit(n.y, n.x, 16);
        *lp<u32x4>(srl, 2 * CS + o) = u32x4{a0, a1, a2, a3};
        *lp<u32x4>(srl, CS + o) = u32x4{d.y, d.z, d.w, n.x};
        *lp<u32x4>(srl, o) = u32x4{a1, a2, a3, a4_};
      }
      CBAR();
      STAMP(3);
      const u32 ab = opq(sr + (u32)((c16 & 3) * CS + 24 - 8 * (c16 >> 2) + 16 * q)), ab8 = opq(ab + 8), bb_ = opq(T + (u32)(32 * c16 + 16 * q));
      auto ldA = [&](int p, int r) { const u32 ad = (u32)(4032 - 512 * p - 64 * r); return cat8(*lp<s16x4>(ab, ad), *lp<s16x4>(ab8, ad)); };
      auto ldB = [&](int m, int r) { return lds128(bb_, (u32)(512 * (m + 1) - 64 * r - 32)); };
      s_wsp = mk_srd(kargs()->w_sp, SEQ * SEQ * 2);
      f32x4 y[8] = {};
#pragma unroll
      for (int c = 0; c < 8; ++c) asm volatile("" : "+v"(y[c]));
      bf16x8 fa[8], fb[8];
#pragma unroll
      for (int p = 7; p >= 0; --p) { fa[p] = ldA(p, 0); fb[7 - p] = ldB(7 - p, 0); }
      sfor<0, 8>([&](auto IR) {
        constexpr int r = decltype(IR)::value;
#pragma unroll
        for (int p = 7; p >= 0; --p) {
#pragma unroll
          for (int m = 0; m + p < 8; ++m) {
            mfma16_ip(y[p + m], fa[p], fb[m]);
            SCHED();
            const int i = (7 - p) * (8 - p) / 2 + m;
            if (p == 0 && r < 7) fb[m] = ldB(m, r + 1);
            if (i % 6 == 0 && 6 * r + i / 6 < 32) a_load(R, s_wsp, a_v, a_sb, (6 * r + i / 6) & 31);
            SCHED();
          }
          if (r < 7) { fa[p] = ldA(p, r + 1); SCHED(); }
        }
      });
      asm volatile("s_nop 7" : "+v"(y[7]), "+v"(y[6]), "+v"(y[5]));
      SCHED();
      STAMP(4);
      const u32 ao = WS_A + (u32)(chn * 4096 + (q & 1) * 512 + c16 * 32 + (q >> 1) * 16);
#pragma unroll
      for (int c = 0; c < 8; c += 2) {
        const auto s0 = __builtin_amdgcn_permlane16_swap(pk2(y[c][0], y[c][1]), pk2(y[c + 1][0], y[c + 1][1]), false, false);
        const auto s1 = __builtin_amdgcn_permlane16_swap(pk2(y[c][2], y[c][3]), pk2(y[c + 1][2], y[c + 1][3]), false, false);
        __builtin_amdgcn_raw_buffer_store_b128(u32x4{s0[0], s1[0], s0[1], s1[1]}, r_ws, ao + c * 512, 0, 16);
      }
      WAITV(4);
      a_all<false>(A, R);
      WAITV(0);
      gflag_st_last(faddr(F_ACNT), (u32)(nch - 1), s_ws, WS_FA + (u32)((tj * 32 + ti) * 256), tag);
      tail<false>(ws, A, z1p, bsp, tag, w, lane, ti, tj TSPASS);
      return;
    }
  }
  __builtin_amdgcn_s_setprio(0);
  s_wsp = mk_srd(kargs()->w_sp, SEQ * SEQ * 2);
  flag_wait<4>(faddr(F_GO), 1);
#pragma unroll
  for (int n = 0; n < 32; ++n) { if (n == 0) STAMP(3); a_load(R, s_wsp, a_v, a_sb, n); }
  STAMP(4);
  a_all(A, R);
  tail<false>(ws, A, z1p, bsp, tag, w, lane, ti, tj TSPASS);
}

extern "C" void kernel_launch(void* const* d_in, const int* in_sizes, int n_in, void* d_out, int out_size, void* d_ws, size_t ws_size,
                              hipStream_t stream) {
  Params P;
  P.u = (const u16*)d_in[0]; P.Are = (const u16*)d_in[1]; P.Aim = (const u16*)d_in[2]; P.Bre = (const u16*)d_in[3];
  P.Bim = (const u16*)d_in[4]; P.Cre = (const u16*)d_in[5]; P.Cim = (const u16*)d_in[6]; P.logdt = (const u16*)d_in[7];
  P.dskip = (const u16*)d_in[8]; P.w_in = (const u16*)d_in[10]; P.w_sp = (const u16*)d_in[11]; P.b_sp = (const u16*)d_in[12];
  P.w_out = (const u16*)d_in[13]; P.gate_w = (const u16*)d_in[14]; P.out = (u16*)d_out; P.ws = (char*)d_ws;
  (void)hipFuncSetAttribute((const void*)fused_kernel, hipFuncAttributeMaxDynamicSharedMemorySize, L_TOTAL);
  fused_kernel<<<256, 512, L_TOTAL, stream>>>(P);
}
```

```cpp
#include <hip/hip_runtime.h>
#include <hip/hip_bf16.h>

typedef unsigned short u16;
typedef unsigned u32;
typedef unsigned long long u64;
typedef short bf16x8 __attribute__((ext_vector_type(8)));
typedef short s16x4 __attribute__((ext_vector_type(4)));
typedef float f32x2 __attribute__((ext_vector_type(2)));
typedef float f32x4 __attribute__((ext_vector_type(4)));
typedef float f32x16 __attribute__((ext_vector_type(16)));
typedef unsigned u32x2 __attribute__((ext_vector_type(2)));
typedef unsigned u32x4 __attribute__((ext_vector_type(4)));
typedef __bf16 bf2_t __attribute__((ext_vector_type(2)));
typedef _Float16 h2_t __attribute__((ext_vector_type(2)));
typedef _Float16 h8_t __attribute__((ext_vector_type(8)));
#define LAS __attribute__((address_space(3)))
#define DEV __device__ __forceinline__
#define SCHED() __builtin_amdgcn_sched_barrier(0)
#define CBAR() asm volatile("" ::: "memory")
#define WAITV(n) asm volatile("s_waitcnt vmcnt(%0)" ::"i"(n) : "memory")
#define WAITL0() asm volatile("s_waitcnt lgkmcnt(0)" ::: "memory")
#define HWBAR() __builtin_amdgcn_s_barrier()

constexpr int SEQ = 2048, DM = 640, NST = 192, FF = 1536, FH = 768;

constexpr u32 WS_EPOCH = 0;
constexpr u32 WS_FZ2 = 4096;
constexpr u32 WS_FA = WS_FZ2 + 256 * 256;
constexpr u32 WS_FUT = WS_FA + 64 * 256;
constexpr u32 WS_FH = WS_FUT + 64 * 256;
constexpr u32 WS_STATS = WS_FH + 4 * 256 * 256;
constexpr u32 WS_Z2 = 1u << 20;
constexpr u32 WS_H = WS_Z2 + SEQ * FH * 2;
constexpr u32 WS_A = WS_H + SEQ * FH * 2;
constexpr u32 WS_UT = WS_A + SEQ * DM * 2;
constexpr u32 WS_END = WS_UT + SEQ * DM * 2;
static_assert(WS_STATS + 32 * 6 * 1024 <= WS_Z2, "ws map");

constexpr u32 L_TOTAL = 163840, L_FLG = L_TOTAL - 256, L_GATE = L_FLG - 256;
constexpr u32 L_X = 0;
constexpr u32 L_RING = 81920, CHUNK = 16384;
constexpr u32 L_GW = 155648;
constexpr u32 L_PART = L_GW + 1280;
constexpr u32 L_RS = L_PART + 3072;
constexpr u32 L_GP = L_RS + 512;
static_assert(L_GP + 1024 <= L_GATE, "lds");
constexpr u32 L_Z1 = 65536;
constexpr u32 L_RED = 0;
constexpr u32 L_W0 = 65536, L_W1 = 114688, L_W1B = 0, L_W2 = 16384, L_W3 = L_W0;
constexpr u32 L_AT = L_W1;
constexpr u32 SC_SZ = 8704, L_T = 8 * SC_SZ, T_SZ = 4608, U_SZ = 4224, L_UA = L_T + 10 * T_SZ;
static_assert(L_UA % 128 == 0 && L_UA + 10 * U_SZ <= L_GATE && SC_SZ % 128 == 0, "lds");
DEV u32 ub_base(int k) { return k < 8 ? k * SC_SZ + 4288 : (k - 8) * SC_SZ + 64; }
enum { F_H3 = 0, F_H3F = 1, F_C0 = 2, F_C1 = 3, F_L2 = 4, F_L3 = 5, F_AT = 6, F_AR = 7, F_XT = 8, F_ACNT = 9, F_P1 = 10, F_P2 = 11, F_P3 = 12, F_C2 = 13, F_UC = 14, F_PC = 15, F_EX = 16 };
#define SPRIO 2
#define ROT4EXPR 0
#define ROTEXPR (ti + 3 * tj) % 10
#define CODE_LINES 1u

#define XST(k)
#define STAMP(k)
#define TSARG
#define TSPASS
#define TSFLUSH()

struct Params {
  const u16 *u, *Are, *Aim, *Bre, *Bim, *Cre, *Cim, *logdt, *dskip, *w_in, *w_sp, *b_sp, *w_out, *gate_w;
  u16* out;
  char* ws;
};

typedef const __attribute__((address_space(4))) Params* KP;
DEV KP kargs() {
  KP p = (KP)__builtin_amdgcn_kernarg_segment_ptr();
  asm volatile("" : "+s"(p));
  return p;
}
DEV float bf2f(u32 v) { return __uint_as_float(v << 16); }
DEV float bfhi(u32 v) { return __uint_as_float(v & 0xffff0000u); }
DEV u32 pk2(float a, float b) { return __builtin_bit_cast(u32, __builtin_convertvector(f32x2{a, b}, bf2_t)); }
DEV u32x4 mk_srd(const void* p, u32 bytes) { return u32x4{(u32)(size_t)p, (u32)((size_t)p >> 32) & 0xffffu, bytes, 0x00020000u}; }
DEV __amdgpu_buffer_rsrc_t mk_rsrc(const void* p, u32 bytes) { return __builtin_amdgcn_make_buffer_rsrc((void*)p, 0, bytes, 0x00020000); }
DEV void dma16(u32x4 srd, u32 voff, u32 soff, u32 dst) {
  asm volatile("s_mov_b32 m0, %2\n\ts_nop 0\n\tbuffer_load_dwordx4 %0, %1, %3 offen lds" ::"v"(voff), "s"(srd), "s"(dst), "s"(soff) : "memory");
}
DEV void m0_set(u32 dst) { asm volatile("s_mov_b32 m0, %0" ::"s"(dst) : "memory"); }
template <int OFF> DEV void dma16o(u32x4 srd, u32 voff, u32 soff) {
  asm volatile("buffer_load_dwordx4 %0, %1, %2 offen offset:%3 lds" ::"v"(voff), "s"(srd), "s"(soff), "i"(OFF) : "memory");
}
DEV void bload(u32x4& d, u32x4 srd, u32 voff, u32 soff) {
  asm volatile("buffer_load_dwordx4 %0, %1, %2, %3 offen" : "=v"(d) : "v"(voff), "s"(srd), "s"(soff) : "memory");
}
DEV void bload_sc1(u32x4& d, u32x4 srd, u32 voff, u32 soff) {
  asm volatile("buffer_load_dwordx4 %0, %1, %2, %3 offen sc1" : "=v"(d) : "v"(voff), "s"(srd), "s"(soff) : "memory");
}
#define WAITV_PIN(n, x) asm volatile("s_waitcnt vmcnt(%1)" : "+v"(x) : "i"(n) : "memory")
template <class T> DEV LAS T* lp(u32 base, u32 off = 0) { return (LAS T*)((LAS char*)(size_t)base + off); }
DEV u32 smask(bool c) { u32 m = __builtin_amdgcn_readfirstlane(c ? 1 : 0); asm volatile("s_nop 0" : "+s"(m)); return m; }
DEV u32 opq(u32 x) { asm volatile("" : "+v"(x)); return x; }
DEV bf16x8 lds128(u32 base, u32 off = 0) { return *lp<bf16x8>(base, off); }
DEV s16x4 ldstr(u32 base, u32 off = 0) { return __builtin_amdgcn_ds_read_tr16_b64_v4i16(lp<s16x4>(base, off)); }
DEV bf16x8 cat8(s16x4 a, s16x4 b) { return __builtin_shufflevector(a, b, 0, 1, 2, 3, 4, 5, 6, 7); }
DEV bf16x8 as8(u32x4 v) { return __builtin_bit_cast(bf16x8, v); }

DEV u32 faddr(int k) { return L_FLG + 4 * k; }
DEV void flag_st(u32 addr, u32 v) { asm volatile("ds_write_b32 %0, %1" ::"v"(addr), "v"(v) : "memory"); }
DEV void flag_inc(u32 addr) {
  asm volatile("s_mov_b64 exec, 1\n\tds_add_u32 %0, %1\n\ts_mov_b64 exec, -1" ::"v"(addr), "v"(1u) : "memory");
}
DEV void flag_pre(u32& pre, u32 addr) { asm volatile("ds_read_b32 %0, %1" : "=v"(pre) : "v"(addr) : "memory"); }
template <int SLEEP = 1> DEV void flag_wait_pre(u32 pre, u32 addr, u32 want) {
  u32 t, cnt, v;
  asm volatile("s_waitcnt lgkmcnt(0)\n\t"
               "v_readfirstlane_b32 %0, %6\n\t"
               "s_cmp_ge_u32 %0, %4\n\t"
               "s_cbranch_scc1 .Lfd%=\n\t"
               "s_mov_b32 %1, 0x20000\n"
               ".Lfw%=:\n\t"
               "s_sleep %5\n\t"
               "ds_read_b32 %2, %3\n\t"
               "s_sub_u32 %1, %1, 1\n\t"
               "s_cbranch_scc1 .Lfe%=\n\t"
               "s_waitcnt lgkmcnt(0)\n\t"
               "v_readfirstlane_b32 %0, %2\n\t"
               "s_cmp_lt_u32 %0, %4\n\t"
               "s_cbranch_scc1 .Lfw%=\n"
               ".Lfe%=:\n\t"
               "s_waitcnt lgkmcnt(0)\n"
               ".Lfd%=:"
               : "=&s"(t), "=&s"(cnt), "=&v"(v) : "v"(addr), "s"(want), "i"(SLEEP), "v"(pre) : "memory", "scc");
}
template <int SLEEP = 1> DEV void flag_wait(u32 addr, u32 want) {
  u32 pre;
  flag_pre(pre, addr);
  flag_wait_pre<SLEEP>(pre, addr, want);
}
DEV void gflag_st(u32x4 srd, u32 off, u32 tag, u32 mask = 1) {
  asm volatile("s_mov_b32 exec_lo, %3\n\ts_mov_b32 exec_hi, 0\n\tbuffer_store_dword %0, off, %1, %2 sc1\n\ts_mov_b64 exec, -1" ::"v"(tag), "s"(srd), "s"(off), "s"(mask) : "memory");
}
DEV void gflag_st_last(u32 addr, u32 last, u32x4 srd, u32 off, u32 tag) {
  u32 old, t;
  u64 m;
  asm volatile("s_mov_b64 exec, 1\n\t"
               "ds_add_rtn_u32 %0, %3, %4\n\t"
               "s_waitcnt lgkmcnt(0)\n\t"
               "v_readfirstlane_b32 %1, %0\n\t"
               "s_cmp_eq_u32 %1, %5\n\t"
               "s_cselect_b64 %2, 1, 0\n\t"
               "s_mov_b64 exec, %2\n\t"
               "buffer_store_dword %8, off, %6, %7 sc1\n\t"
               "s_mov_b64 exec, -1"
               : "=&v"(old), "=&s"(t), "=&s"(m) : "v"(addr), "v"(1u), "s"(last), "s"(srd), "s"(off), "v"(tag) : "memory", "scc");
}
DEV void gflag_wait(u32x4 srd, u32 voff, u32 tag) {
  u32 v, cnt;
  asm volatile("s_mov_b32 %1, 0x4000\n"
               ".Lgw%=:\n\t"
               "buffer_load_dword %0, %2, %3, 0 offen sc1\n\t"
               "s_waitcnt vmcnt(0)\n\t"
               "v_cmp_ne_u32 vcc, %4, %0\n\t"
               "s_cbranch_vccz .Lgd%=\n\t"
               "s_sleep 2\n\t"
               "s_sub_u32 %1, %1, 1\n\t"
               "s_cbranch_scc0 .Lgw%=\n"
               ".Lgd%=:"
               : "=&v"(v), "=&s"(cnt) : "v"(voff), "s"(srd), "s"(tag) : "memory", "scc", "vcc");
}

DEV void gflag_check(u32 v0, u32x4 srd, u32 voff, u32 tag) {
  u32 v, cnt;
  asm volatile("v_cmp_ne_u32 vcc, %4, %5\n\t"
               "s_cbranch_vccz .Lgd%=\n\t"
               "s_mov_b32 %1, 0x4000\n"
               ".Lgw%=:\n\t"
               "buffer_load_dword %0, %2, %3, 0 offen sc1 ; half 1\n\t"
               "s_waitcnt vmcnt(0) ; half 1\n\t"
               "v_cmp_ne_u32 vcc, %4, %0\n\t"
               "s_cbranch_vccz .Lgd%=\n\t"
               "s_sleep 2\n\t"
               "s_sub_u32 %1, %1, 1\n\t"
               "s_cbranch_scc0 .Lgw%=\n"
               ".Lgd%=:"
               : "=&v"(v), "=&s"(cnt) : "v"(voff), "s"(srd), "s"(tag), "v"(v0) : "memory", "scc", "vcc");
}

DEV float gelu_f(float a) {
  const float k0 = -2.3022081999f, k1 = -0.1029432397f;
  const float e = __builtin_amdgcn_exp2f(a * __builtin_fmaf(a * a, k1, k0));
  return a * __builtin_amdgcn_rcpf(1.0f + e);
}
template <int CTRL> DEV float dppf(float x) { return __builtin_bit_cast(float, __builtin_amdgcn_update_dpp(0, __builtin_bit_cast(int, x), CTRL, 0xf, 0xf, false)); }
#define MFMA16(a, b, c) __builtin_amdgcn_mfma_f32_16x16x32_bf16(a, b, c, 0, 0, 0)
#define MFMA32H(a, b, c) __builtin_amdgcn_mfma_f32_32x32x16_f16(a, b, c, 0, 0, 0)

DEV f32x4 rowsum16(const float (&v)[16]) {
  float a[8], o0, o1, o2, o3;
#define RS1(d, x, y) "v_add_f32_dpp " d ", " x ", " x " row_ror:8 row_mask:0xf bank_mask:0x3\n\tv_add_f32_dpp " d ", " y ", " y " row_ror:8 row_mask:0xf bank_mask:0xc\n\t"
  asm volatile("s_nop 1\n\t" RS1("%0", "%8", "%16") RS1("%1", "%9", "%17") RS1("%2", "%10", "%18") RS1("%3", "%11", "%19")
               RS1("%4", "%12", "%20") RS1("%5", "%13", "%21") RS1("%6", "%14", "%22") RS1("%7", "%15", "%23")
               : "=&v"(a[0]), "=&v"(a[1]), "=&v"(a[2]), "=&v"(a[3]), "=&v"(a[4]), "=&v"(a[5]), "=&v"(a[6]), "=&v"(a[7])
               : "v"(v[0]), "v"(v[1]), "v"(v[2]), "v"(v[3]), "v"(v[4]), "v"(v[5]), "v"(v[6]), "v"(v[7]),
                 "v"(v[8]), "v"(v[9]), "v"(v[10]), "v"(v[11]), "v"(v[12]), "v"(v[13]), "v"(v[14]), "v"(v[15]));
#define RS2(d, x, y) "v_add_f32_dpp " d ", " x ", " x " row_half_mirror row_mask:0xf bank_mask:0x5\n\tv_add_f32_dpp " d ", " y ", " y " row_half_mirror row_mask:0xf bank_mask:0xa\n\t"
#define RS3(qp) "v_add_f32_dpp %0, %0, %0 quad_perm:" qp " row_mask:0xf bank_mask:0xf\n\tv_add_f32_dpp %1, %1, %1 quad_perm:" qp " row_mask:0xf bank_mask:0xf\n\t" \
                "v_add_f32_dpp %2, %2, %2 quad_perm:" qp " row_mask:0xf bank_mask:0xf\n\tv_add_f32_dpp %3, %3, %3 quad_perm:" qp " row_mask:0xf bank_mask:0xf\n\t"
  asm volatile("s_nop 1\n\t" RS2("%0", "%4", "%8") RS2("%1", "%5", "%9") RS2("%2", "%6", "%10") RS2("%3", "%7", "%11") RS3("[1,0,3,2]") RS3("[2,3,0,1]")
               : "=&v"(o0), "=&v"(o1), "=&v"(o2), "=&v"(o3)
               : "v"(a[0]), "v"(a[1]), "v"(a[2]), "v"(a[3]), "v"(a[4]), "v"(a[5]), "v"(a[6]), "v"(a[7]));
  return f32x4{o0, o1, o2, o3};
}
DEV void mfma16_ip(f32x4& c, bf16x8 a, bf16x8 b) { asm volatile("v_mfma_f32_16x16x32_bf16 %0, %1, %2, %0" : "+v"(c) : "v"(a), "v"(b)); }
DEV void mfma16_z(f32x4& c, bf16x8 a, bf16x8 b) { asm volatile("v_mfma_f32_16x16x32_bf16 %0, %1, %2, 0" : "=&v"(c) : "v"(a), "v"(b)); }
DEV h2_t hmul(h2_t x, h2_t e) { return __builtin_elementwise_fma(h2_t{x.y, x.y}, h2_t{-e.y, e.x}, h2_t{x.x, x.x} * e); }
DEV h2_t toh(f32x2 v) { return __builtin_convertvector(v, h2_t); }
DEV f32x2 csq(f32x2 e) {
  float t, x, y;
  asm("v_mul_f32 %0, %3, %3\n\tv_add_f32 %1, %2, %2\n\tv_fma_f32 %0, %2, %2, -%0\n\tv_mul_f32 %1, %1, %3" : "=&v"(x), "=&v"(y) : "v"(e.x), "v"(e.y));
  (void)t;
  return f32x2{x, y};
}
DEV f32x2 cexp2(float ar, float ai, float l) {
  const float m = __builtin_amdgcn_exp2f(ar * l), th = __builtin_amdgcn_fractf(ai * l);
  return f32x2{__uint_as_float(opq(__float_as_uint(m * __builtin_amdgcn_cosf(th)))), m * __builtin_amdgcn_sinf(th)};
}

struct TUnit { u32 t[4][4], o[4][4]; };
#define TSTEP_ASM(qp) \
  "s_mov_b32 vcc_lo, %24\n\ts_mov_b32 vcc_hi, %24\n\t" \
  "v_cndmask_b32_dpp %0, %16, %8, vcc quad_perm:" qp " row_mask:0xf bank_mask:0xf\n\t" \
  "v_cndmask_b32_dpp %1, %17, %9, vcc quad_perm:" qp " row_mask:0xf bank_mask:0xf\n\t" \
  "v_cndmask_b32_dpp %2, %18, %10, vcc quad_perm:" qp " row_mask:0xf bank_mask:0xf\n\t" \
  "v_cndmask_b32_dpp %3, %19, %11, vcc quad_perm:" qp " row_mask:0xf bank_mask:0xf\n\t" \
  "v_cndmask_b32_dpp %4, %20, %12, vcc quad_perm:" qp " row_mask:0xf bank_mask:0xf\n\t" \
  "v_cndmask_b32_dpp %5, %21, %13, vcc quad_perm:" qp " row_mask:0xf bank_mask:0xf\n\t" \
  "v_cndmask_b32_dpp %6, %22, %14, vcc quad_perm:" qp " row_mask:0xf bank_mask:0xf\n\t" \
  "v_cndmask_b32_dpp %7, %23, %15, vcc quad_perm:" qp " row_mask:0xf bank_mask:0xf"
#define TSTEP_OPS(o, g0, g1, s, x) \
  : "=&v"(o[g0][0]), "=&v"(o[g0][1]), "=&v"(o[g0][2]), "=&v"(o[g0][3]), "=&v"(o[g1][0]), "=&v"(o[g1][1]), "=&v"(o[g1][2]), "=&v"(o[g1][3]) \
  : "v"(s[g0][0]), "v"(s[g0][1]), "v"(s[g0][2]), "v"(s[g0][3]), "v"(s[g1][0]), "v"(s[g1][1]), "v"(s[g1][2]), "v"(s[g1][3]), \
    "v"(s[(g0) xor (x)][0]), "v"(s[(g0) xor (x)][1]), "v"(s[(g0) xor (x)][2]), "v"(s[(g0) xor (x)][3]), "v"(s[(g1) xor (x)][0]), "v"(s[(g1) xor (x)][1]), "v"(s[(g1) xor (x)][2]), "v"(s[(g1) xor (x)][3])
DEV void tstep(TUnit& U, const u32x4 (&r)[4], int k) {
  if (k == 0) asm volatile(TSTEP_ASM("[1,0,3,2]") TSTEP_OPS(U.t, 0, 2, r, 1), "i"(0x55555555u) : "vcc");
  if (k == 1) asm volatile(TSTEP_ASM("[1,0,3,2]") TSTEP_OPS(U.t, 1, 3, r, 1), "i"(0xAAAAAAAAu) : "vcc");
  if (k == 2) asm volatile(TSTEP_ASM("[2,3,0,1]") TSTEP_OPS(U.o, 0, 1, U.t, 2), "i"(0x33333333u) : "vcc");
  if (k == 3) asm volatile(TSTEP_ASM("[2,3,0,1]") "\n\ts_nop 1" TSTEP_OPS(U.o, 2, 3, U.t, 2), "i"(0xCCCCCCCCu) : "vcc");
}
DEV u32x4 tout(const TUnit& U, int g) { return u32x4{U.o[g][0], U.o[g][1], U.o[g][2], U.o[g][3]}; }

template <int V> struct IC { static constexpr int value = V; };
template <int I, int N, class F> DEV void sfor(F&& f) {
  if constexpr (I < N) { f(IC<I>{}); sfor<I + 1, N>(f); }
}
#define BARRIER() do { WAITL0(); HWBAR(); SCHED(); } while (0)

template <class F, class G>
DEV void kgen(f32x16& kacc, const u32x4 (&praw)[3], const u32 (&px)[6], float ldt, float ldtx, u32 sr, u32 T, int lane, F&& shared_done, G&& early) {
  const float dt0 = bf2f(pk2(expf(ldt), 0.f) & 0xffffu), dtx = bf2f(pk2(expf(ldtx), 0.f) & 0xffffu);
  const u32 rdp = opq(sr + (u32)((lane & 31) * 272 + (lane >> 5) * 16));
#pragma unroll
  for (int k = 0; k < 3; ++k) *lp<u32x4>(T + 1024 + lane * 16, k * 1024) = praw[k];
  CBAR();
  const u32 prd = opq(T + 1024 + lane * 2);
  m0_set(sr);
  auto put = [&](int row, h2_t v) { asm volatile("ds_write_addtid_b32 %0 offset:%1" ::"v"(v), "i"(row * 272) : "memory"); };
  kacc = f32x16{};
  float ar[4], ai[4], wr[4], wi[4], mx[2] = {0.f, 0.f}, wsc[2], kinv[2];
#pragma unroll
  for (int ps = 0; ps < 4; ++ps) {
    u32 pw[6];
#pragma unroll
    for (int k = 0; k < 6; ++k) pw[k] = ps ? (u32)*lp<u16>(prd, (k >> 1) * 1024 + (k & 1) * 384 + (ps - 1) * 128) : (lane < 48 ? px[k] : 0u);
    const float dt = ps ? dt0 : dtx;
    ar[ps] = (dt * bf2f(pw[0])) * 1.4426950409f;
    ai[ps] = (dt * bf2f(pw[1])) * 0.15915494309f;
    const float br = dt * bf2f(pw[2]), bi = dt * bf2f(pw[3]), cr = bf2f(pw[4]), ci = bf2f(pw[5]);
    wr[ps] = __uint_as_float(opq(__float_as_uint(__builtin_fmaf(cr, br, -ci * bi))));
    wi[ps] = -__builtin_fmaf(cr, bi, ci * br);
    mx[ps == 0] = fmaxf(mx[ps == 0], fmaxf(fabsf(wr[ps]), fabsf(wi[ps])));
  }
#pragma unroll
  for (int k = 0; k < 2; ++k) {
    float m = mx[k];
    m = fmaxf(m, dppf<0x128>(m)); m = fmaxf(m, dppf<0x124>(m)); m = fmaxf(m, dppf<0x122>(m)); m = fmaxf(m, dppf<0x121>(m));
    const u32 mb = __float_as_uint(m);
    const u32 ex = min(max(max(max((u32)__builtin_amdgcn_readlane(mb, 0), (u32)__builtin_amdgcn_readlane(mb, 16)),
                               max((u32)__builtin_amdgcn_readlane(mb, 32), (u32)__builtin_amdgcn_readlane(mb, 48))) >> 23, 1u), 252u);
    wsc[k] = __uint_as_float((253u - ex) << 23);
    kinv[k] = __uint_as_float((ex + 1u) << 23);
  }
  h2_t gw[4];
#pragma unroll
  for (int ps = 0; ps < 4; ++ps) gw[ps] = toh(f32x2{__uint_as_float(opq(__float_as_uint(wr[ps] * wsc[ps == 0]))), wi[ps] * wsc[ps == 0]});
  h2_t c1, c2, c4, c256, c512, c1024, g0, qv[32];
  auto setup = [&]() {
    const f32x2 e1 = cexp2(ar[0], -ai[0], 1.f), e8 = cexp2(ar[0], ai[0], 8.f), e256 = cexp2(ar[0], -ai[0], 256.f);
    const f32x2 e2 = csq(e1), e16 = csq(e8), e512 = csq(e256), e4 = csq(e2), e32 = csq(e16), e1024 = csq(e512), e64 = csq(e32), e128 = csq(e64);
    c1 = toh(e1); c2 = toh(e2); c4 = toh(e4); c256 = toh(e256); c512 = toh(e512); c1024 = toh(e1024);
    qv[1] = toh(e8); qv[2] = toh(e16); qv[4] = toh(e32); qv[8] = toh(e64); qv[16] = toh(e128);
    g0 = gw[0];
#pragma unroll
    for (int k = 0; k < 3; ++k) { ar[k] = ar[k + 1]; ai[k] = ai[k + 1]; gw[k] = gw[k + 1]; }
  };
  qv[0] = h2_t{(_Float16)1.f, (_Float16)0.f};
  setup();
  SCHED();
#pragma unroll
  for (int ps = 0; ps < 4; ++ps) {
    early(ps);
    SCHED();
    {
      const h2_t p1 = hmul(g0, c1), p2 = hmul(g0, c2), p4 = hmul(g0, c4), g1 = hmul(g0, c256), g2 = hmul(g0, c512), g4 = hmul(g0, c1024);
      const h2_t p3 = hmul(p1, c2), p258 = hmul(g1, c2), g3 = hmul(g1, c512), p5 = hmul(p1, c4);
      SCHED();
      put(0, g0); put(1, p1); put(2, p2); put(4, p4); put(8, g1); put(12, g2); put(16, g4);
      SCHED();
      const h2_t p6 = hmul(p2, c4), p260 = hmul(g1, c4), p516 = hmul(g2, c4), g5 = hmul(g1, c1024), g6 = hmul(g2, c1024), g8 = hmul(g4, c1024);
      const h2_t p7 = hmul(p3, c4), p262 = hmul(p258, c4), p772 = hmul(g3, c4), g7 = hmul(g3, c1024);
      SCHED();
      put(3, p3); put(9, p258); put(14, g3); put(5, p5); put(6, p6); put(10, p260); put(13, p516);
      SCHED();
      qv[3] = hmul(qv[1], qv[2]); qv[5] = hmul(qv[1], qv[4]); qv[6] = hmul(qv[2], qv[4]); qv[24] = hmul(qv[8], qv[16]);
      qv[9] = hmul(qv[1], qv[8]); qv[10] = hmul(qv[2], qv[8]); qv[12] = hmul(qv[4], qv[8]); qv[7] = hmul(qv[3], qv[4]);
      SCHED();
      put(17, g5); put(18, g6); put(20, g8); put(7, p7); put(11, p262); put(15, p772); put(19, g7);
    }
    h8_t pf[8], qf[8];
#pragma unroll
    for (int kk = 0; kk < 8; ++kk) pf[kk] = *lp<h8_t>(rdp, kk * 32);
    CBAR();
    SCHED();
    qv[11] = hmul(qv[3], qv[8]); qv[13] = hmul(qv[5], qv[8]); qv[14] = hmul(qv[6], qv[8]); qv[15] = hmul(qv[7], qv[8]);
    SCHED();
#pragma unroll
    for (int j = 0; j < 8; ++j) put(j, qv[j]);
    SCHED();
#pragma unroll
    for (int j = 1; j < 8; ++j) qv[16 + j] = hmul(qv[j], qv[16]);
    SCHED();
#pragma unroll
    for (int j = 8; j < 16; ++j) put(j, qv[j]);
    SCHED();
#pragma unroll
    for (int j = 1; j < 8; ++j) qv[24 + j] = hmul(qv[j], qv[24]);
    SCHED();
#pragma unroll
    for (int j = 16; j < 32; ++j) put(j, qv[j]);
#pragma unroll
    for (int kk = 0; kk < 8; ++kk) qf[kk] = *lp<h8_t>(rdp, kk * 32);
    CBAR();
    SCHED();
    if (ps == 1) {
#pragma unroll
      for (int r = 0; r < 12; ++r) kacc[r] = __uint_as_float(opq(__float_as_uint(kacc[r] * kinv[1])));
      shared_done(kacc);
      kacc = f32x16{};
    }
    SCHED();
    if (ps < 3) setup();
#pragma unroll
    for (int kk = 0; kk < 8; ++kk) kacc = MFMA32H(pf[kk], qf[kk], kacc);
    __builtin_amdgcn_sched_group_barrier(0x402, 20, 0);
#pragma unroll
    for (int kk = 0; kk < 8; ++kk) { __builtin_amdgcn_sched_group_barrier(0x8, 1, 0); __builtin_amdgcn_sched_group_barrier(0x402, 5, 0); }
    SCHED();
  }
#pragma unroll
  for (int r = 0; r < 12; ++r) kacc[r] = __uint_as_float(opq(__float_as_uint(kacc[r] * kinv[0])));
}
DEV void expand(f32x16 kacc, float dsk, u32 CB, u32 T, int lane) {
  const int bq = lane & 31, hh = lane >> 5;
  kacc[0] += __uint_as_float(opq(lane == 0 ? __float_as_uint(dsk) : 0u));
  *lp<u32x2>(T + lane * 8) = u32x2{0u, 0u};
  *lp<u32x2>(T + 512 + (u32)(bq * 16 + hh * 8)) = u32x2{pk2(kacc[0], kacc[1]), pk2(kacc[2], kacc[3])};
  *lp<u32>(CB + (u32)(hh ? 256 + 4 * bq : 8 * bq)) = pk2(kacc[4], kacc[5]);
  *lp<u32>(CB + (u32)(hh ? 384 + 4 * bq : 8 * bq + 4)) = pk2(kacc[6], kacc[7]);
#pragma unroll
  for (int r = 0; r < 4; ++r) *lp<u16>(CB + (u32)(512 + 64 * (4 * hh + r) + 2 * bq)) = (u16)pk2(kacc[8 + r], 0.f);
  CBAR();
  {
    const u32x2 v = *lp<u32x2>(CB + (u32)(8 * bq));
    const u32 n4 = *lp<u16>(CB + (u32)(8 * bq + 8));
    const u32x4 o = {__builtin_amdgcn_perm(v.x, v.x, 0x01000100u), __builtin_amdgcn_perm(v.y, v.x, 0x05040302u),
                     __builtin_amdgcn_perm(v.y, v.y, 0x01000100u), __builtin_amdgcn_perm(n4, v.y, 0x05040302u)};
    *lp<u32x4>(T + 1024 + (u32)(bq * 16)) = o;
  }
  {
    const u32 v = *lp<u32>(CB + (u32)(256 + 4 * lane));
    const u32 n2 = *lp<u16>(CB + (u32)(256 + 4 * lane + 4));
    *lp<u32x4>(T + 1536 + (u32)(lane * 16)) = u32x4{__builtin_amdgcn_perm(v, v, 0x01000100u), v, __builtin_amdgcn_perm(v, v, 0x03020302u), n2 * 0x10001u};
  }
#pragma unroll
  for (int g = 0; g < 2; ++g) {
    const u32 v0 = *lp<u16>(CB + (u32)(512 + 2 * (g * 64 + lane))), v1 = *lp<u16>(CB + (u32)(514 + 2 * (g * 64 + lane)));
    *lp<u32x4>(T + 2560 + (u32)((g * 64 + lane) * 16)) = u32x4{v0 * 0x10001u, v0 * 0x10001u, ((lane & 1) ? v1 : v0) | (v1 << 16), v1 * 0x10001u};
  }
  CBAR();
}
DEV void prm_load(KP kp, u32x4 (&praw)[3], u32& ldt, u32& dsk, int chn, int lane) {
  const int l24 = lane < 24 ? lane : (lane < 48 ? lane - 24 : 0);
  const size_t po = (size_t)chn * NST + l24 * 8;
  praw[0] = *(const u32x4*)((lane < 24 ? kp->Are : kp->Aim) + po);
  praw[1] = *(const u32x4*)((lane < 24 ? kp->Bre : kp->Bim) + po);
  praw[2] = *(const u32x4*)((lane < 24 ? kp->Cre : kp->Cim) + po);
  ldt = kp->logdt[chn];
  dsk = kp->dskip[chn];
}
DEV void u_fetch(char* ws, int k, int chn, int lane) {
  const __amdgpu_buffer_rsrc_t r_ws = mk_rsrc(ws, WS_END);
  const u32 ra = opq(L_UA + k * U_SZ + lane * 16), wb = opq(ub_base(k) + lane * 16);
  u32x4 d[4];
  u32 n[4];
#pragma unroll
  for (int g = 0; g < 4; ++g) d[g] = __builtin_amdgcn_raw_buffer_load_b128(r_ws, WS_UT + (u32)(chn * 4096 + lane * 16), g * 1024, 16);
  *lp<u32>((lane < 32 ? L_UA + k * U_SZ : ub_base(k)) + 4096 + (lane & 15) * 4) = 0u;
#pragma unroll
  for (int g = 0; g < 4; ++g) *lp<u32x4>(ra, g * 1024) = d[g];
  CBAR();
#pragma unroll
  for (int g = 0; g < 4; ++g) n[g] = *lp<u32>(ra, g * 1024 + 16);
#pragma unroll
  for (int g = 0; g < 4; ++g)
    *lp<u32x4>(wb, g * 1024) = u32x4{__builtin_amdgcn_alignbit(d[g].y, d[g].x, 16), __builtin_amdgcn_alignbit(d[g].z, d[g].y, 16),
                                     __builtin_amdgcn_alignbit(d[g].w, d[g].z, 16), __builtin_amdgcn_alignbit(n[g], d[g].w, 16)};
  CBAR();
}
template <int TM> DEV void conv(char* ws, int k, int chn, u32 tag, int sidx, int lane) {
  constexpr int NT = TM >= 64 ? 8 : 6;
  const int c16 = lane & 15, q = lane >> 4;
  u32 ab[4], bb = opq(L_T + k * T_SZ + (u32)(32 * c16 + 16 * q + 480));
#pragma unroll
  for (int h = 0; h < 4; ++h) ab[h] = opq(((c16 & 1) ? L_UA + k * U_SZ : ub_base(k)) + (u32)(3548 - 1024 * h - 4 * (c16 >> 1) + 16 * q));
  auto ldA = [&](int p) {
    const u32 o = (p & 1) ? 0 : 512;
    return u32x4{*lp<u32>(ab[p >> 1], o), *lp<u32>(ab[p >> 1], o + 4), *lp<u32>(ab[p >> 1], o + 8), *lp<u32>(ab[p >> 1], o + 12)};
  };
  f32x4 y[8] = {};
#pragma unroll
  for (int c = 0; c < 8; ++c) asm volatile("" : "+v"(y[c]));
  u32x4 fa[8];
  bf16x8 fb[8];
#pragma unroll
  for (int p = NT - 1; p >= 0; --p) { fa[p] = ldA(p); fb[NT - 1 - p] = lds128(bb, 512 * (NT - 1 - p)); }
#pragma nounroll
  for (int r = 0; r < 8; ++r) {
    bb -= 64;
#pragma unroll
    for (int h = 0; h < 4; ++h) ab[h] -= 64;
    SCHED();
#pragma unroll
    for (int p = NT - 1; p >= 0; --p) {
#pragma unroll
      for (int m = 0; m + p < NT; ++m) {
        if ((TM >> (p + m)) & 1) mfma16_ip(y[p + m], as8(fa[p]), fb[m]);
        if (p == 0) { SCHED(); fb[m] = lds128(bb, 512 * m); SCHED(); }
      }
      SCHED();
      fa[p] = ldA(p);
      SCHED();
    }
  }
  asm volatile("s_nop 7" : "+v"(y[0]), "+v"(y[1]), "+v"(y[2]), "+v"(y[3]), "+v"(y[4]), "+v"(y[5]), "+v"(y[6]), "+v"(y[7]));
  const __amdgpu_buffer_rsrc_t r_ws = mk_rsrc(ws, WS_END);
  const u32 ao = WS_A + (u32)(chn * 4096 + (q & 1) * 512 + c16 * 32 + (q >> 1) * 16);
#pragma unroll
  for (int c = 0; c < 8; c += 2) {
    if ((TM >> c) & 1) {
      const auto s0 = __builtin_amdgcn_permlane16_swap(pk2(y[c][0], y[c][1]), pk2(y[c + 1][0], y[c + 1][1]), false, false);
      const auto s1 = __builtin_amdgcn_permlane16_swap(pk2(y[c][2], y[c][3]), pk2(y[c + 1][2], y[c + 1][3]), false, false);
      __builtin_amdgcn_raw_buffer_store_b128(u32x4{s0[0], s1[0], s0[1], s1[1]}, r_ws, ao + c * 512, 0, 16);
    }
  }
}
DEV void ssm_wg(char* ws, u32 ep_raw, u32 pf_raw, int sidx, int w, int lane TSARG) {
  const int c0 = 10 * sidx, cx = c0 + 8 + (w >> 2), c16 = lane & 15, q = lane >> 4;
  const KP kp = kargs();
  const u32x4 s_ws = mk_srd(ws, WS_END);
  const u32 sc = L_UA + (w & 3) * 10240;
  const int tch = 320 * (sidx & 1) + 80 * (w & 3);
  u32x4 praw[3];
  u32 ldt, dsk, px[6];
  prm_load(kp, praw, ldt, dsk, c0 + w, lane);
  {
    const int o = cx * NST + 48 * (w & 3) + min(lane, 47);
    px[0] = kp->Are[o]; px[1] = kp->Aim[o]; px[2] = kp->Bre[o]; px[3] = kp->Bim[o]; px[4] = kp->Cre[o]; px[5] = kp->Cim[o];
  }
  const u32 ldtx = kp->logdt[cx], dskx = kp->dskip[c0 + 8 + (w & 1)];
  if (w >= 4) {
    const u32x4 s_x = mk_srd(kp->u, SEQ * DM * 2);
#pragma unroll
    for (int u = 0; u < 5; ++u) {
      const int off = u * 1024 + lane * 16;
      const u32 vo = (u32)((off / 160) * 1280 + (off % 160) + (sidx >> 1) * 64 * 1280 + tch * 2);
      dma16(s_x, vo, 0, sc + u * 1024);
      dma16(s_x, vo, 32 * 1280, sc + 5120 + u * 1024);
    }
  }
  BARRIER();
  u32 tag;
  asm volatile("s_waitcnt vmcnt(0)\n\tv_readfirstlane_b32 %0, %1\n\ts_nop 1" : "=s"(tag) : "v"(ep_raw), "v"(pf_raw) : "memory");
  tag += 1u;
  if (w >= 4) {
    __builtin_amdgcn_s_setprio(3);
    const u32 hr = opq(sc + (u32)((8 * q + 3 - ((lane >> 2) & 3)) * 160 + 8 * (lane & 3)));
    bf16x8 tv[2][5];
#pragma unroll
    for (int g = 0; g < 2; ++g)
#pragma unroll
      for (int e = 0; e < 5; ++e) tv[g][e] = cat8(ldstr(hr, g * 5120 + 640 + 32 * e), ldstr(hr, g * 5120 + 32 * e));
    CBAR();
#pragma unroll
    for (int g = 0; g < 2; ++g) {
      const u32 tw = opq(sc + (u32)(c16 * 128 + (((7 - 4 * g - q) ^ (c16 & 7)) * 16)));
#pragma unroll
      for (int e = 0; e < 5; ++e) *lp<bf16x8>(tw, e * 2048) = tv[g][e];
    }
    CBAR();
    const __amdgpu_buffer_rsrc_t r_ws = mk_rsrc(ws, WS_END);
    const u32 tr = opq(sc + (u32)((lane >> 3) * 128 + (((lane & 7) ^ (lane >> 3)) * 16)));
    const u32 uo = WS_UT + (u32)((tch + (lane >> 3)) * 4096 + (248 - 8 * (sidx >> 1) + (lane & 7)) * 16);
#pragma unroll
    for (int k = 0; k < 10; ++k) __builtin_amdgcn_raw_buffer_store_b128(*lp<u32x4>(tr, k * 1024), r_ws, uo + (u32)(k * 8 * 4096), 0, 16);
  } else {
    __builtin_amdgcn_s_setprio(2);
  }
  STAMP(1);
  f32x16 kacc;
  f32x4 kp4[3];
  u32 pfw[7] = {};
  kgen(kacc, praw, px, bf2f(ldt), bf2f(ldtx), w * SC_SZ, L_T + w * T_SZ, lane, [&](const f32x16& kx) {
    kp4[0] = f32x4{kx[0], kx[1], kx[2], kx[3]}; kp4[1] = f32x4{kx[4], kx[5], kx[6], kx[7]}; kp4[2] = f32x4{kx[8], kx[9], kx[10], kx[11]};
  }, [&](int ps) {
    if (ps == 0 && w >= 4) { WAITV(0); gflag_st_last(faddr(F_UC), 3, s_ws, WS_FUT + (u32)(sidx * 256), tag); __builtin_amdgcn_s_setprio(1); }
    if (ps < 2) {
      const u32x4 s_p = mk_srd(kargs()->w_sp, SEQ * SEQ * 2);
      const u32 vo = (u32)((sidx >> 4) * 512 * 4096 + (((sidx & 7) * 8 + w) * 64 + lane) * 128);
      asm volatile("buffer_load_dword %0, %2, %3, %4 offen\n\tbuffer_load_dword %1, %2, %3, %5 offen"
                   : "=&v"(pfw[2 * ps]), "=&v"(pfw[2 * ps + 1]) : "v"(vo), "s"(s_p), "s"(ps * 1048576u), "s"(ps * 1048576u + 524288u) : "memory");
    }
    if (ps == 0) {
      const u32 ln = (u32)(((sidx & 7) * 8 + w) * 64 + lane);
      asm volatile("buffer_load_dword %0, %1, %2, 0 offen" : "=v"(pfw[4]) : "v"((ln / 6) * 3072 + (ln % 6) * 128 + ((sidx >> 3) & 1) * 768), "s"(mk_srd(kargs()->w_in, DM * FF * 2)) : "memory");
    }
    if (ps >= 2) {
      const u32 ln = (u32)(((sidx & 7) * 8 + w) * 36 + min(lane, 35) + (ps - 2) * 2304);
      asm volatile("buffer_load_dword %0, %1, %2, 0 offen" : "=v"(pfw[3 + ps]) : "v"((ln / 6) * 1280 + (ln % 6) * 128 + ((sidx >> 3) & 1) * 512), "s"(mk_srd(kargs()->w_out, FH * DM * 2)) : "memory");
    }
  });
#pragma unroll
  for (int k = 0; k < 3; ++k) *lp<f32x4>(w * SC_SZ + 1024 + lane * 16, k * 1024) = kp4[k];
  flag_inc(faddr(F_PC));
  expand(kacc, bf2f(dsk), w * SC_SZ, L_T + w * T_SZ, lane);
  STAMP(2);
  gflag_wait(s_ws, WS_FUT + (u32)((2 * (lane & 31) + (sidx >> 5)) * 256), tag);
  asm volatile("" ::"v"(pfw[0]), "v"(pfw[1]), "v"(pfw[2]), "v"(pfw[3]), "v"(pfw[4]), "v"(pfw[5]), "v"(pfw[6]));
  flag_wait<2>(faddr(F_UC), 4);
  STAMP(3);
  u_fetch(ws, w, c0 + w, lane);
  __builtin_amdgcn_s_setprio(0);
  STAMP(4);
  conv<0xFF>(ws, w, c0 + w, tag, sidx, lane);
  STAMP(5);
  if (w < 4) {
    if (w < 2) {
      __builtin_amdgcn_s_setprio(2);
      flag_wait<2>(faddr(F_PC), 8);
#pragma unroll
      for (int k = 0; k < 3; ++k) {
        const u32 pb = (u32)(w * 4 * SC_SZ + 1024 + k * 1024 + lane * 16);
        const f32x4 v = (*lp<f32x4>(pb) + *lp<f32x4>(pb, SC_SZ)) + (*lp<f32x4>(pb, 2 * SC_SZ) + *lp<f32x4>(pb, 3 * SC_SZ));
#pragma unroll
        for (int r = 0; r < 4; ++r) kacc[4 * k + r] = v[r];
      }
      flag_inc(faddr(F_PC));
      expand(kacc, bf2f(dskx), w * SC_SZ, L_T + (w + 8) * T_SZ, lane);
      flag_wait<2>(faddr(F_PC), 10);
      u_fetch(ws, w + 8, c0 + w + 8, lane);
      flag_inc(faddr(F_XT));
      __builtin_amdgcn_s_setprio(0);
    }
    flag_wait<4>(faddr(F_XT), 2);
    if (w & 1) conv<0xC3>(ws, 8 + (w >> 1), c0 + 8 + (w >> 1), tag, sidx, lane);
    else conv<0x3C>(ws, 8 + (w >> 1), c0 + 8 + (w >> 1), tag, sidx, lane);
  }
  WAITV(0);
  gflag_st_last(faddr(F_ACNT), 7, s_ws, WS_FA + (u32)(sidx * 256), tag);
  STAMP(6);
  TSFLUSH();
}

DEV void a_load(u32x4 (&R)[4][4], u32x4 srd, u32 voff, u32 sbase, int n) {
  bload(R[n >> 2][n & 3], srd, voff, sbase + (u32)((32 * (n >> 3) + 4 * ((n >> 2) & 1) + (n & 3)) * 4096));
}
DEV void a_take(bf16x8 (&A)[8][4], const TUnit& U, int un) {
#pragma unroll
  for (int g = 0; g < 4; ++g) A[4 * (un >> 2) + g][un & 3] = as8(tout(U, g));
}
constexpr int NPRE = 12;
#define POLL_IT 12
#define NA_LO 10
constexpr int na(int i) { return i == 9 ? 10 : (i >= NA_LO && i <= NA_LO + 2) ? 2 : 0; }
constexpr int TU0 = NA_LO < 9 ? 10 : 12;
constexpr int nd(int i) { return (i >= 10 && i <= 17 ? 1 : 0) + (i >= 16 ? 2 : 0); }
constexpr int a_first(int i) { return i <= 0 ? 0 : a_first(i - 1) + na(i - 1); }
constexpr int n_lead(int i) { return i == POLL_IT ? 1 : i == POLL_IT + 2 ? 2 : 0; }
constexpr int n_dma(int i) { return i < 6 ? 3 : i + 4 < 20 ? 2 : 0; }
constexpr int issued(int it) { return it <= 0 ? NPRE : issued(it - 1) + n_lead(it - 1) + n_dma(it - 1) + na(it - 1) + nd(it - 1); }
constexpr int w_end(int c) { return c < 4 ? 3 * (c + 1) : issued(c - 4) + n_lead(c - 4) + n_dma(c - 4); }
constexpr int a_end(int n) {
  int i = 0;
  while (a_first(i + 1) <= n) ++i;
  return issued(i) + n_lead(i) + n_dma(i) + n - a_first(i) + 1;
}
constexpr int imin(int a, int b) { return a < b ? a : b; }
static_assert(a_first(20) == 16, "schedule");
#define WINSZ 12
constexpr int WIN = WINSZ;
static_assert(WIN <= 13, "h must be older than the loads waited for at its flag");
constexpr int N_POLL = 46, N_H0 = 58;
constexpr bool wo_at(int n) { return n >= 52 && n < 63; }
constexpr int z_younger(int n) {
  int c = (n + WIN - 1 < 63 ? n + WIN - 1 : 63) - n + (n > N_POLL && n <= N_POLL + WIN ? 1 : 0) + (n > N_H0 ? n - N_H0 : 0);
  for (int k = n - WIN; k < n; ++k) c += wo_at(k);
  return c;
}
static_assert(N_H0 == 58 && WIN == 12 && N_POLL + WIN < 64, "schedule");

DEV void wo_piece(u32x4 s_wo, int lane, int tj, int rot4, int pl, int jj, int u, u32 dst) {
  const int c16 = lane & 15, q = lane >> 4;
  dma16(s_wo, (u32)(q * 1280 + tj * 256 + (c16 ^ (2 * (q + 4 * (u & 1)))) * 16), (u32)((128 * jj + 32 * ((pl + rot4) & 3) + 4 * u) * 1280), dst + u * 1024);
}
DEV void tail3(char* ws, u32 tag, int ti, int tj, int w, int lane, u32x4 (&h0)[6], u32 pv TSARG) {
  const int c16 = lane & 15, q = lane >> 4, a4 = (lane >> 2) & 3, p4 = lane & 3, rot4 = ROT4EXPR;
  const __amdgpu_buffer_rsrc_t r_ws = mk_rsrc(ws, WS_END);
  const u32x4 s_ws = mk_srd(ws, WS_END), s_wo = mk_srd(kargs()->w_out, FH * DM * 2);
  const u32 fh3 = WS_FH + (u32)(((3 * 32 + ti) * 8 + tj) * 256);
  const u32 fh = WS_FH + (u32)((ti * 8 + min(lane & 7, 5)) * 256);
  if (tj == 5 || w >= 6) {
    WAITV(0);
    gflag_st_last(faddr(F_H3F), 7, s_ws, fh3, tag);
    STAMP(9);
    if (tj < 5 && w == 6) {
      gflag_wait(s_ws, fh + 2 * 65536, tag);
      flag_st(faddr(F_P2), 1);
      gflag_wait(s_ws, WS_FA + (u32)(min(128 * tj / 10 + (lane & 15), (128 * tj + 127) / 10) * 256), tag);
      u32x4 av[16];
#pragma unroll
      for (int k = 0; k < 16; ++k)
        av[k] = __builtin_amdgcn_raw_buffer_load_b128(r_ws, WS_A + (u32)((tj * 128 + (lane >> 3)) * 4096 + ti * 128 + (lane & 7) * 16), k * 8 * 4096, 16);
      flag_wait(faddr(F_C1), 4);
      const u32 aw = opq(L_AT + (u32)((lane >> 3) * 128 + (((lane & 7) ^ (lane >> 3)) * 16)));
#pragma unroll
      for (int k = 0; k < 16; ++k) *lp<u32x4>(aw, k * 1024) = av[k];
      flag_st(faddr(F_AT), 1);
    } else if (tj < 5) {
      gflag_wait(s_ws, fh + 3 * 65536, tag);
      flag_st(faddr(F_P3), 1);
      if (blockIdx.x == 64) {
        gflag_wait(s_ws, WS_FA + (u32)(lane * 256), tag);
        gflag_st(s_ws, WS_EPOCH, tag);
      }
    }
    STAMP(10);
  } else if (w >= 4) {
    u32 lv[4];
#pragma unroll
    for (int u = 0; u < 4; ++u) lv[u] = (u32)((16 * (w & 1) + 4 * u + q) * 1280 + tj * 256 + (c16 ^ (2 * (q + 4 * (u & 1)))) * 16 - u * 1024);
#pragma unroll
    for (int k = 0; k < 13; ++k) {
      if (k == 3) flag_wait(faddr(F_H3), 8);
      if (k == 2) { WAITV(8); gflag_st_last(faddr(F_H3F), 7, s_ws, fh3, tag); STAMP(9); }
      if (k == 7) flag_wait(faddr(F_C0), 4);
      if (k == 9) { WAITV(8); flag_inc(faddr(F_L2)); }
      m0_set((k == 0 ? L_W1B : (k < 7 ? L_W2 : L_W3) + ((k - 1) % 6) * 8192) + (w & 1) * 4096);
      u32 so = (u32)((128 * (k == 0 ? 5 : (k - 1) % 6) + 32 * (((k == 0 ? 1 : k < 7 ? 2 : 3) + rot4) & 3)) * 1280);
      asm volatile("s_nop 0" : "+s"(so));
      dma16o<0>(s_wo, lv[0], so);
      dma16o<1024>(s_wo, lv[1], so);
      dma16o<2048>(s_wo, lv[2], so);
      dma16o<3072>(s_wo, lv[3], so);
    }
    WAITV(0);
    flag_inc(faddr(F_L3));
    STAMP(10);
  } else {
    const int kk = w >> 1, mt = 2 * (w & 1) + kk;
    const u32 hb = WS_H + (u32)(ti * 4 * 24576 + kk * 3072 + lane * 16);
    u32x4 hf[4][6];
    auto hload = [&](int p) {
#pragma unroll
      for (int j = 0; j < 6; ++j) hf[p][j] = __builtin_amdgcn_raw_buffer_load_b128(r_ws, hb, (u32)((mt ^ (j & 1)) * 24576 + p * 6144 + (j >> 1) * 1024), 16);
    };
#pragma unroll
    for (int j = 0; j < 6; ++j) hf[0][j] = h0[j];
    f32x4 o[2][8];
    const u32 wrd = (u32)((4 * q + a4) * 256 + (((4 * q + a4) & 7) * 32) + 8 * p4);
    u32 ad[3][8];
#pragma unroll
    for (int e = 0; e < 8; ++e) {
      ad[0][e] = opq((wrd ^ (u32)(32 * e)) + L_W0 + kk * 24576);
      ad[1][e] = opq((wrd ^ (u32)(32 * e)) + L_W2 + kk * 24576);
      ad[2][e] = opq((wrd ^ (u32)(32 * e)) + (kk ? L_W1B : L_W1 + 16384));
    }
    s16x4 wh[2][8][2];
    auto w_rd = [&](int n, int k) {
      const int p = n / 3, i = n % 3;
      const u32 off = n == 5 ? 0u : (u32)((p == 1 ? L_W1 - L_W0 : 0) + i * 8192);
      wh[n & 1][k >> 1][k & 1] = ldstr(ad[n == 5 ? 2 : p == 2 ? 1 : 0][k >> 1], off + (k & 1) * 4096);
    };
    gflag_check(pv, s_ws, fh + 65536, tag);
    hload(1);
    XST(1);
#pragma unroll
    for (int k = 0; k < 16; ++k) w_rd(0, k);
    sfor<0, 12>([&](auto IN) {
      constexpr int n = decltype(IN)::value;
      if (n == 1) { WAITV(6); gflag_st_last(faddr(F_H3F), 7, s_ws, fh3, tag); XST(2); }
      if (n == 2) { flag_inc(faddr(F_C0)); flag_wait(faddr(F_H3), 8); }
      if (n == 4) { flag_inc(faddr(F_C1)); flag_wait(faddr(F_P2), 1); hload(2); XST(3); flag_wait(faddr(F_L2), 2); XST(4); }
      if (n == 7) { flag_wait(faddr(F_P3), 1); hload(3); XST(5); }
      if (n == 8) { flag_inc(faddr(F_C2)); flag_wait(faddr(F_L3), 2); XST(6); }
      if (n == 11) flag_wait(faddr(F_C2), 4);
      SCHED();
#pragma unroll
      for (int k = 0; k < 16; ++k) {
        const int m = n == 11 ? k < 8 : k & 1, e = n == 11 ? k & 7 : k >> 1;
        if (n) mfma16_ip(o[m][e], cat8(wh[n & 1][e][0], wh[n & 1][e][1]), as8(hf[n / 3][2 * (n % 3) + m]));
        else mfma16_z(o[m][e], cat8(wh[0][e][0], wh[0][e][1]), as8(hf[0][m]));
        SCHED();
        if (n < 11) w_rd(n + 1, k);
        else if (k >= 8) *lp<f32x4>(L_W2 + w * 8192 + lane * 16, e * 1024) = o[1][e];
        SCHED();
      }
    });
    flag_st(faddr(F_EX) + 4 * w, 1);
    asm volatile("s_nop 4" : "+v"(o[0][5]), "+v"(o[0][6]), "+v"(o[0][7]));
    XST(7);
    flag_wait(faddr(F_AT), 1);
    u32x2 at[8];
    {
      const u32 ab = opq(L_AT + (u32)((4 * q + a4) * 128 + (((4 * (mt >> 1) + p4) ^ ((4 * q + a4) & 7)) * 16) + 8 * (mt & 1)));
#pragma unroll
      for (int e = 0; e < 8; ++e) at[e] = __builtin_bit_cast(u32x2, ldstr(ab, 2048 * e));
    }
    const float gate = *lp<float>(L_GATE + (u32)((mt * 16 + c16) * 4));
    flag_wait(faddr(F_EX) + 4 * (w ^ 2), 1);
    const u32 ot = L_W2 + (w ^ 2) * 8192;
#pragma unroll
    for (int e = 0; e < 8; ++e) o[0][e] += *lp<f32x4>(ot + lane * 16, e * 1024);
    CBAR();
    const u32 ob = ot + (u32)(c16 * 256 + 8 * (q & 1));
#pragma unroll
    for (int e = 0; e < 8; ++e)
      *lp<u32x2>(ob + (u32)(((2 * e + (q >> 1)) ^ c16) * 16)) =
          u32x2{pk2(__builtin_fmaf(gate, bf2f(at[e].x & 0xffffu), o[0][e][0]), __builtin_fmaf(gate, bfhi(at[e].x), o[0][e][1])),
                pk2(__builtin_fmaf(gate, bf2f(at[e].y & 0xffffu), o[0][e][2]), __builtin_fmaf(gate, bfhi(at[e].y), o[0][e][3]))};
    CBAR();
    const __amdgpu_buffer_rsrc_t r_out = mk_rsrc(kargs()->out, SEQ * DM * 2);
#pragma unroll
    for (int k = 0; k < 4; ++k) {
      const u32x4 v = *lp<u32x4>(ot + (u32)((4 * k + q) * 256 + ((c16 ^ (4 * k + q)) * 16)));
      __builtin_amdgcn_raw_buffer_store_b128(v, r_out, (u32)((ti * 64 + 32 * (mt >> 1) + 8 * k + 4 * (mt & 1) + q) * 1280 + tj * 256 + c16 * 16), 0, 0);
    }
    STAMP(11);
  }
  TSFLUSH();
}

DEV void gemm_wg(char* ws, u32 ep_raw, u32 pf_raw, int ti, int tj, int w, int lane TSARG) {
  const int c16 = lane & 15, q = lane >> 4, a4 = (lane >> 2) & 3, p4 = lane & 3, mh = w & 1, nq = w >> 1;
  const __amdgpu_buffer_rsrc_t r_ws = mk_rsrc(ws, WS_END);
  const u32x4 s_ws = mk_srd(ws, WS_END);
  const KP kp = kargs();
  const u32x4 s_x = mk_srd(kp->u, SEQ * DM * 2), s_w = mk_srd(kp->w_in, DM * FF * 2), s_wsp = mk_srd(kp->w_sp, SEQ * SEQ * 2);
  const int rot = ROTEXPR, rot4 = ROT4EXPR;
  auto line_of = [&](int l) { const int r = l + rot; return (u32)(r >= 10 ? r - 10 : r); };
  const int xr = 8 * (w & 1) + (lane >> 3);
  const u32 x_v = (u32)((ti * 64 + 32 * (w >> 2) + 8 * (xr >> 2) + 4 * ((w >> 1) & 1) + (xr & 3)) * 1280 + (((lane & 7) ^ (xr >> 1)) * 16));
  const u32 w_v0 = (u32)((8 * w + q) * 3072 + ((c16 ^ (2 * (q + 4 * (w & 1)))) * 16) + tj * 256), w_v1 = w_v0 + 4 * 3072 - 1024;
  auto x_issue = [&](int l) { dma16(s_x, x_v, line_of(l) * 128, L_X + l * 8192 + w * 1024); };
  auto w_issue = [&](int c) {
    m0_set(L_RING + (c & 3) * CHUNK + w * 2048);
    u32 so = line_of(c % 10) * (64 * 3072) + (c < 10 ? FH * 2 : 0);
    asm volatile("s_nop 0" : "+s"(so));
    dma16o<0>(s_w, w_v0, so);
    dma16o<1024>(s_w, w_v1, so);
  };
  const u32 trow = (u32)(32 * mh + 8 * (c16 >> 2) + (c16 & 3));
  u32 bsp_raw;
  {
    asm volatile("buffer_load_ushort %0, %1, %2, 0 offen" : "=v"(bsp_raw)
                 : "v"((u32)((ti * 64 + 32 * (w >> 2) + 8 * (c16 >> 2) + 4 * ((w >> 1) & 1) + (c16 & 3)) * 2)), "s"(mk_srd(kp->b_sp, SEQ * 2)) : "memory");
    dma16(mk_srd(kp->gate_w, DM * 2), lane * 16, (w & 1) * 256, L_GW + (w & 1) * 256);
  }
#pragma unroll
  for (int l = 0; l < 4; ++l) { x_issue(l); w_issue(l); }
  STAMP(1);
  const u32 rd0 = L_X + (u32)(mh * 4096 + (c16 >> 3) * 1024 + (c16 & 7) * 128 + ((q ^ (c16 >> 1)) * 16));
  const u32 xb[2][2] = {{opq(rd0), opq(rd0 ^ 64)}, {opq(rd0 + 40960), opq((rd0 ^ 64) + 40960)}};
  const u32 wrow = L_RING + (u32)((8 * q + a4) * 256 + 8 * p4), wf = (u32)(a4 + 4 * (q & 1));
  u32 wb[2] = {opq(wrow + ((2 * nq) ^ wf) * 32), opq(wrow + ((2 * nq + 1) ^ wf) * 32)};
  const u32 a_v = (u32)(8 * a4 * 4096 + (4 * q + p4) * 16);
  const int ksl = (w + ti + tj) & 7;
  const u32 a_sb = (u32)(ti * 64 * 4096 + ksl * 512 + (tj & 1) * 256);
  const u32 d_v0 = (u32)((8 * (lane >> 5) + ((lane >> 3) & 3)) * 4096 + (((lane & 7) ^ (lane >> 4)) * 16)), d_v1 = (d_v0 ^ 64) + 16 * 4096;
  bf16x8 Gf[2][2];
  const u32 gxb = opq((rd0 ^ (u32)((nq >> 1) * 64)) + (nq & 1) * 2048), gwb = opq(L_GW + (u32)(q * 16 + (nq >> 1) * 64));
  auto g_read = [&](int c) { Gf[c & 1][0] = lds128(gwb + line_of(c - 10) * 128); Gf[c & 1][1] = lds128(c < 18 ? gxb : gxb + 65536, ((c - 10) & 7) * 8192); };
  bf16x8 Xf[2][2][2];
  s16x4 Wh[2][2][2][2];
  auto f_read = [&](int c, int k) {
    const int l = c % 10;
    if (k < 4) Xf[c & 1][k >> 1][k & 1] = lds128(xb[l >= 5][k >> 1], (l % 5) * 8192 + (k & 1) * 2048);
    else { const int j = k - 4; Wh[c & 1][j >> 2][(j >> 1) & 1][j & 1] = ldstr(wb[(j >> 1) & 1], (c & 3) * CHUNK + (j >> 2) * 8192 + (j & 1) * 1024); }
  };
  auto Wf = [&](int c, int s, int e) { return cat8(Wh[c & 1][s][e][0], Wh[c & 1][s][e][1]); };
  u32x4 R[4][4];
  bf16x8 A[8][4];
  TUnit U;
  u32x4 gs, s_wo = mk_srd(kp->w_out, FH * DM * 2);
  if (tj == 5) s_wo.z = 0;
  auto wo_first = [&](int k) { wo_piece(s_wo, lane, tj, rot4, k / 6, k % 6, w, (k < 6 ? L_W0 : L_W1 - 6 * 8192) + k * 8192); };
  const u32 st_v = WS_STATS + (u32)((ti * 6 + min(w, 5)) * 1024 + lane * 16);
  f32x4 acc2[2][2] = {}, acc1[2][2] = {}, ag = {};
  asm volatile("" : "+v"(acc2[0][0]), "+v"(acc2[0][1]), "+v"(acc2[1][0]), "+v"(acc2[1][1]));
  u32 tag;
  float bsp;
  asm volatile("s_waitcnt vmcnt(%5)\n\tv_readfirstlane_b32 %0, %2\n\tv_lshlrev_b32 %1, 16, %3\n\ts_nop 1"
               : "=s"(tag), "=v"(bsp) : "v"(ep_raw), "v"(bsp_raw), "v"(pf_raw), "i"(NPRE - 3) : "memory");
  tag += 1u;
  BARRIER();
#pragma unroll
  for (int k = 0; k < 12; ++k) f_read(0, k);
  sfor<0, 20>([&](auto IT) {
    constexpr int it = decltype(IT)::value, af = a_first(it), tu = (it - TU0) >> 1;
    constexpr int c0 = (it < 19 && it != 9) ? issued(it) - w_end(it + 1) : 63;
    constexpr int c1 = (it >= TU0 && it < TU0 + 8 && !(it & 1)) ? issued(it) - a_end(4 * tu + 3) : 63;
    constexpr int c2 = it == POLL_IT + 4 ? issued(it) - issued(POLL_IT + 2) - 2 : 63;
    constexpr int cw = imin(c0, imin(c1, c2)), cw10 = issued(10) - w_end(10), cws = issued(POLL_IT + 2) - issued(POLL_IT) - 1;
    static_assert(cw >= 0 && cw < 64 && cws < 64, "wait count");
    if (cw < 63) WAITV(cw);
    __builtin_amdgcn_s_waitcnt(0xC07F);
    HWBAR();
    SCHED();
    if (it == POLL_IT + 4) { gflag_st(s_ws, WS_FZ2 + (u32)((tj * 32 + ti) * 256), tag, smask(w == 0)); STAMP(5); }
    if (it == POLL_IT) bload_sc1(gs, s_ws, st_v, 0);
    if (it == POLL_IT + 2) {
      STAMP(4);
      f32x2 t = *lp<f32x2>(L_PART + lane * 8);
#pragma unroll
      for (int j = 1; j < 6; ++j) t += *lp<f32x2>(L_PART + lane * 8, 512 * j);
      const float mean = t.x * (1.0f / FH);
      const float rsd = __builtin_amdgcn_rsqf(fmaxf(__builtin_fmaf(-mean, mean, t.y * (1.0f / FH)), 0.f) + 1e-5f);
      *lp<f32x2>(L_RS + lane * 8) = f32x2{rsd, -mean * rsd};
      CBAR();
      const u32 rb = opq(L_RS + (u32)((32 * mh + 8 * q) * 8));
      f32x4 rs[4];
#pragma unroll
      for (int k = 0; k < 4; ++k) rs[k] = *lp<f32x4>(rb, 16 * k);
      const u32 zo = WS_Z2 + (u32)(((tj * 8 + 2 * nq) * 64 + 4 * (ti >> 1) + q) * 1024 + (c16 + 16 * (2 * (ti & 1) + mh)) * 16);
#pragma unroll
      for (int e = 0; e < 2; ++e) {
        u32 o[4];
#pragma unroll
        for (int k = 0; k < 4; ++k)
          o[k] = pk2(__builtin_fmaf(acc2[k >> 1][e][2 * (k & 1)], rs[k][0], rs[k][1]), __builtin_fmaf(acc2[k >> 1][e][2 * (k & 1) + 1], rs[k][2], rs[k][3]));
        __builtin_amdgcn_raw_buffer_store_b128(u32x4{o[0], o[1], o[2], o[3]}, r_ws, zo + e * 65536, 0, 16);
      }
      SCHED();
    }
    if (it == 9) {
      const u32 blk = (u32)(2 * ((nq + rot4) & 3));
      asm volatile("" : "+v"(acc1[0][0]), "+v"(acc1[0][1]), "+v"(acc1[1][0]), "+v"(acc1[1][1]), "+v"(ag));
      wb[0] = opq(wrow + (blk ^ wf) * 32);
      wb[1] = opq(wrow + ((blk + 1) ^ wf) * 32);
    }
    const u32 wso = line_of((it + 4) % 10) * (64 * 3072) + (it + 4 < 10 ? FH * 2 : 0);
    auto vmem = [&](int j) {
      const int jw = j - (it < 6 ? 1 : 0), ja = j - n_dma(it);
      if (it < 6 && j == 0) x_issue(it + 4);
      else if (j < n_dma(it) && jw == 0) { m0_set(L_RING + (it & 3) * CHUNK + w * 2048); asm volatile("s_nop 0"); dma16o<0>(s_w, w_v0, wso); }
      else if (j < n_dma(it)) dma16o<1024>(s_w, w_v1, wso);
      else if (ja < na(it)) a_load(R, s_wsp, a_v, a_sb, (af + ja) & 15);
      else if (ja - na(it) < nd(it)) {
        const int ja = j - n_dma(it) - na(it);
        const bool inx = it <= 17 && ja == 0;
        const int pc = inx ? it - 10 : 8 + 2 * (it - 16) + ja - (it <= 17 ? 1 : 0);
        dma16(s_wsp, (pc & 1) ? d_v1 : d_v0, (a_sb ^ 256u) + (u32)((32 * (pc >> 3) + 4 * ((pc >> 2) & 1)) * 4096 + ((pc >> 1) & 1) * 128),
              inx ? L_X + pc * 8192 + w * 1024 : L_RING + (it & 3) * CHUNK + w * 2048 + (pc & 1) * 1024);
      }
    };
    SCHED();
#pragma unroll
    for (int k = 0; k < 8; ++k) {
      const int s = k >> 2, b = (k >> 1) & 1, e = k & 1;
      if (it < 10) mfma16_ip(acc2[b][e], Xf[it & 1][s][b], Wf(it, s, e));
      else mfma16_ip(acc1[b][e], Wf(it, s, e), Xf[it & 1][s][b]);
      SCHED();
      if (it < 19 && it != 9 && k < 6) { f_read(it + 1, 2 * k); f_read(it + 1, 2 * k + 1); }
      if (!(k & 1)) vmem(k >> 1);
      if (k == 7 && it != 9) vmem(4);
      if (it >= 10 && it < 19 && k == 5) g_read(it + 1);
      if (it >= TU0 && it < TU0 + 8 && k >= 6) {
        tstep(U, R[tu & 3], 2 * (it & 1) + k - 6);
        if ((it & 1) && k == 7) a_take(A, U, tu & 3);
      }
      SCHED();
    }
    if (it >= 10) mfma16_ip(ag, Gf[it & 1][0], Gf[it & 1][1]);
    if (it == 9) {
      STAMP(2);
      float sv[16];
      asm volatile("s_nop 7" : "+v"(acc2[0][0]), "+v"(acc2[0][1]), "+v"(acc2[1][0]), "+v"(acc2[1][1]));
#pragma unroll
      for (int k = 0; k < 8; ++k) {
        const float g0 = gelu_f(acc2[k >> 2][0][k & 3]), g1 = gelu_f(acc2[k >> 2][1][k & 3]);
        acc2[k >> 2][0][k & 3] = g0;
        acc2[k >> 2][1][k & 3] = g1;
        sv[2 * k] = g0 + g1;
        sv[2 * k + 1] = __builtin_fmaf(g0, g0, g1 * g1);
        if (k & 1) { a_load(R, s_wsp, a_v, a_sb, (af + 2 + (k >> 1)) & 15); SCHED(); }
      }
      *lp<f32x4>(L_PART + (u32)(nq * 512 + (32 * mh + 8 * q) * 8 + a4 * 16)) = rowsum16(sv);
      SCHED();
      WAITV(cw10 - 4);
      BARRIER();
      if (w == 0) {
        const u32 pb = L_PART + lane * 8;
        const f32x2 t = (*lp<f32x2>(pb) + *lp<f32x2>(pb, 512)) + (*lp<f32x2>(pb, 1024) + *lp<f32x2>(pb, 1536));
        __builtin_amdgcn_raw_buffer_store_b128(u32x4{__float_as_uint(t.x), tag, __float_as_uint(t.y), tag}, r_ws, WS_STATS + (u32)((ti * 6 + tj) * 1024 + lane * 16), 0, 16);
      }
#pragma unroll
      for (int k = 0; k < 12; ++k) f_read(10, k);
      g_read(10);
#pragma unroll
      for (int k = 6; k < 10; ++k) a_load(R, s_wsp, a_v, a_sb, (af + k) & 15);
      STAMP(3);
    }
    if (it == POLL_IT + 1) {
      WAITV_PIN(cws, gs);
      u32x4 g = gs;
#pragma nounroll
      for (u32 sp = 0; sp < 0x4000u && !__all(g.y == tag && g.w == tag); ++sp) {
        __builtin_amdgcn_s_sleep(2);
        g = __builtin_amdgcn_raw_buffer_load_b128(r_ws, st_v, 0, 16);
      }
      *lp<u32x2>(L_PART + (u32)(min(w, 5) * 512 + lane * 8)) = u32x2{g.x, g.z};
    }
  });
  asm volatile("s_nop 7" : "+v"(acc1[0][0]), "+v"(acc1[0][1]), "+v"(acc1[1][0]), "+v"(acc1[1][1]), "+v"(ag));
  STAMP(6);
  gflag_wait(s_ws, WS_FZ2 + (u32)((tj * 32 + 4 * ksl + (lane & 3)) * 256), tag);
  STAMP(7);
  {
    const u32 db = (u32)((q >> 1) * 16384 + (c16 & 7) * 128 + ((4 * (q & 1)) ^ (c16 >> 1)) * 16);
#pragma unroll
    for (int e = 0; e < 4; ++e) {
      const u32 ax = opq((db ^ (u32)(16 * e)) + L_X + w * 1024 + (c16 >> 3) * 8192), ar = opq((db ^ (u32)(16 * e)) + L_RING + w * 2048 + (c16 >> 3) * 1024);
      A[4 + e][0] = lds128(ax); A[4 + e][1] = lds128(ax, 32768); A[4 + e][2] = lds128(ar); A[4 + e][3] = lds128(ar, 32768);
    }
  }
  const u32 zb = WS_Z2 + (u32)((tj * 8 * 64 + 8 * ksl) * 1024 + lane * 16);
  u32x4 win[WIN];
  auto zload = [&](int n) { bload_sc1(win[n % WIN], s_ws, zb, (u32)((((n >> 4) + rot4) & 3) * 131072 + ((n >> 3) & 1) * 65536 + ((n & 7) ^ (4 * (tj & 1))) * 1024)); };
  *lp<float>(L_GP + (nq >> 1) * 256 + trow * 4 + (nq & 1) * 16) = ag[0];
#pragma unroll
  for (int k = 0; k < 4; ++k) {
    const int b = k >> 1, e = k & 1;
    zload(3 * k); zload(3 * k + 1); zload(3 * k + 2);
    *lp<u32x2>(L_Z1 + (u32)((2 * mh * 4 + nq) * 1024 + lane * 8), b * 4096 + e * 512) =
        u32x2{pk2(gelu_f(acc1[b][e][0]), gelu_f(acc1[b][e][1])), pk2(gelu_f(acc1[b][e][2]), gelu_f(acc1[b][e][3]))};
    SCHED();
  }
  const u32 rwb = opq(L_RED + (u32)(w * 4096 + lane * 16));
  const u32 rrb = opq(L_RED + (u32)((lane >> 5) * 16384 + (w >> 1) * 1024 + (32 * (w & 1) + (lane & 31)) * 16));
  f32x4 rv[4], se;
  u32x2 z1p[4];
  float og;
  auto z1_fetch = [&]() {
#pragma unroll
    for (int p = 0; p < 4; ++p) z1p[p] = *lp<u32x2>(L_Z1 + (u32)((w >> 1) * 4096 + (lane >> 5) * 512 + (32 * (w & 1) + (lane & 31)) * 8), p * 1024);
    const u32 gb = L_GP + (u32)((32 * (w >> 2) + 8 * (c16 >> 2) + 4 * ((w >> 1) & 1) + (c16 & 3)) * 4);
    rv[0][0] = *lp<float>(gb);
    rv[0][1] = *lp<float>(gb, 256);
  };
  auto gate_fin = [&]() {
    const float gate = __builtin_amdgcn_rcpf(1.0f + __builtin_amdgcn_exp2f((rv[0][0] + rv[0][1]) * -1.4426950409f));
    *lp<float>(L_GATE + (u32)(((w >> 1) * 16 + c16) * 4)) = gate;
    og = 1.0f - gate;
    bsp *= og;
    asm volatile("" : "+v"(og), "+v"(bsp));
  };
  const u32 hoff = lane < 32 ? WS_H + (u32)(((ti * 4 + (w >> 1)) * 4 * 6 + tj) * 1024 + (32 * (w & 1) + lane) * 16) : WS_END;
  auto red_ld = [&](int e) {
#pragma unroll
    for (int k = 0; k < 4; ++k) rv[k] = *lp<f32x4>(rrb, (e & 1) * 32768 + k * 4096);
  };
  auto red_add = [&](int e) {
    const f32x4 t = (rv[0] + rv[1]) + (rv[2] + rv[3]);
    if (!(e & 1)) { se = t; asm volatile("" : "+v"(se)); return; }
    float g[4];
#pragma unroll
    for (int r = 0; r < 4; ++r) {
      const auto x = __builtin_amdgcn_permlane32_swap(__float_as_uint(se[r]), __float_as_uint(t[r]), false, false);
      g[r] = __builtin_fmaf(__uint_as_float(x[0]) + __uint_as_float(x[1]), og, bsp);
    }
    const u32x2 z = z1p[e >> 1];
    const u32 o0 = pk2(bf2f(z.x & 0xffffu) * g[0], bfhi(z.x) * g[1]), o1 = pk2(bf2f(z.y & 0xffffu) * g[2], bfhi(z.y) * g[3]);
    const auto y0 = __builtin_amdgcn_permlane32_swap(o0, o0, false, false), y1 = __builtin_amdgcn_permlane32_swap(o1, o1, false, false);
    __builtin_amdgcn_raw_buffer_store_b128(u32x4{y0[0], y1[0], y0[1], y1[1]}, r_ws, hoff + (u32)((e >> 1) * 6144), 0, 16);
  };
  const u32 w0mask = smask(w == 0);
  f32x4 acc[2][4];
  u32x4 h0[6], s_h = s_ws;
  if (w >= 4 || tj == 5) s_h.z = 0;
  u32 pv;
  const u32 fh0 = WS_FH + (u32)((ti * 8 + min(lane & 7, 5)) * 256);
  sfor<0, 64>([&](auto IN) {
    constexpr int n = decltype(IN)::value, e = n >> 3, ks = n & 7;
    constexpr int zy = z_younger(n);
    WAITV_PIN(zy, win[n % WIN]);
    const bf16x8 zf = as8(win[n % WIN]);
#pragma unroll
    for (int m = 0; m < 4; ++m) { if (ks) mfma16_ip(acc[e & 1][m], zf, A[ks][m]); else mfma16_z(acc[e & 1][m], zf, A[ks][m]); }
    SCHED();
    if (n + WIN < 64) zload(n + WIN);
    if (e > 0 && ks < 4) *lp<f32x4>(rwb, ((e - 1) & 1) * 32768 + ks * 1024) = acc[(e - 1) & 1][ks & 3];
    if (ks == 5) {
      WAITL0();
      HWBAR();
      if (e == 4 || e == 6) gflag_st(s_ws, WS_FH + (u32)((((e - 4) / 2 * 32 + ti) * 8 + tj) * 256), tag, w0mask);
      if (e > 0) red_ld(e - 1); else z1_fetch();
    }
    if (wo_at(n)) wo_first((n + 3) % 11);
    if (n == N_POLL) asm volatile("buffer_load_dword %0, %1, %2, 0 offen sc1" : "=v"(pv) : "v"(fh0), "s"(s_ws) : "memory");
    if (n == N_H0) { WAITV(63 - N_POLL - WIN + 1 + 7); gflag_check(pv, s_ws, fh0, tag); }
    if (n >= N_H0) bload_sc1(h0[(n + 60 - N_H0) % 6], s_h, (rwb & 1023u), WS_H + (u32)((ti * 4 + ((2 * (w & 1) + ((w >> 1) & 1)) ^ ((n - N_H0) & 1))) * 24576 + ((w >> 1) & 1) * 3072 + ((n - N_H0) >> 1) * 1024));
    if (ks == 7) { if (e > 0) red_add(e - 1); else gate_fin(); }
    SCHED();
  });
  asm volatile("s_nop 3" : "+v"(acc[1][0]), "+v"(acc[1][1]), "+v"(acc[1][2]), "+v"(acc[1][3]));
#pragma unroll
  for (int m = 0; m < 4; ++m) *lp<f32x4>(rwb, 32768 + m * 1024) = acc[1][m];
  asm volatile("buffer_load_dword %0, %1, %2, %3 offen sc1" : "=v"(pv) : "v"(fh0), "s"(s_ws), "s"(65536u) : "memory");
  WAITV(12);
  BARRIER();
  STAMP(8);
  red_ld(7);
  gflag_st(s_ws, WS_FH + (u32)(((2 * 32 + ti) * 8 + tj) * 256), tag, w0mask);
  red_add(7);
  asm volatile("s_waitcnt vmcnt(1)" : "+v"(h0[0]), "+v"(h0[1]), "+v"(h0[2]), "+v"(h0[3]), "+v"(h0[4]), "+v"(h0[5]), "+v"(pv)::"memory");
  flag_inc(faddr(F_H3));
  tail3(ws, tag, ti, tj, w, (int)opq(lane), h0, pv TSPASS);
}

__global__ __launch_bounds__(512, 2) void fused_kernel(Params P_unused) {
  extern __shared__ __attribute__((aligned(16))) char smem[];
  const int lane = threadIdx.x & 63;
  const int w = __builtin_amdgcn_readfirstlane(threadIdx.x >> 6);
  const int b = blockIdx.x, xcd = b & 7, idx = b >> 3;
  u32 pf_raw, ep_raw;
  {
    u64 pc;
    asm volatile("s_getpc_b64 %0" : "=s"(pc));
    const u32 ln = (u32)(((idx & 7) * 8 + w) * 8 + (lane & 7));
    asm volatile("global_load_dword %0, %1, %2" : "=v"(pf_raw) : "v"((ln < CODE_LINES ? ln : 0u) * 128u), "s"(pc) : "memory");
  }
  char* ws = kargs()->ws;
  STAMP(0);
  if (w == 4) *lp<u32>(L_FLG + 4 * lane) = 0;
  asm volatile("buffer_load_dword %0, off, %1, 0 sc1" : "=v"(ep_raw) : "s"(mk_srd(ws, WS_END)) : "memory");
  if (idx < 8) ssm_wg(ws, ep_raw, pf_raw, xcd * 8 + idx, w, lane TSPASS);
  else gemm_wg(ws, ep_raw, pf_raw, (xcd >> 1) * 8 + (idx - 8) / 3, (xcd & 1) * 3 + (idx - 8) % 3, w, lane TSPASS);
}

extern "C" void kernel_launch(void* const* d_in, const int* in_sizes, int n_in, void* d_out, int out_size, void* d_ws, size_t ws_size,
                              hipStream_t stream) {
  Params P;
  P.u = (const u16*)d_in[0]; P.Are = (const u16*)d_in[1]; P.Aim = (const u16*)d_in[2]; P.Bre = (const u16*)d_in[3];
  P.Bim = (const u16*)d_in[4]; P.Cre = (const u16*)d_in[5]; P.Cim = (const u16*)d_in[6]; P.logdt = (const u16*)d_in[7];
  P.dskip = (const u16*)d_in[8]; P.w_in = (const u16*)d_in[10]; P.w_sp = (const u16*)d_in[11]; P.b_sp = (const u16*)d_in[12];
  P.w_out = (const u16*)d_in[13]; P.gate_w = (const u16*)d_in[14]; P.out = (u16*)d_out; P.ws = (char*)d_ws;
  (void)hipFuncSetAttribute((const void*)fused_kernel, hipFuncAttributeMaxDynamicSharedMemorySize, L_TOTAL);
  fused_kernel<<<256, 512, L_TOTAL, stream>>>(P);
}
```

```cpp
#include <hip/hip_runtime.h>
#include <hip/hip_bf16.h>

typedef unsigned short u16;
typedef unsigned u32;
typedef unsigned long long u64;
typedef short bf16x8 __attribute__((ext_vector_type(8)));
typedef short s16x4 __attribute__((ext_vector_type(4)));
typedef float f32x2 __attribute__((ext_vector_type(2)));
typedef float f32x4 __attribute__((ext_vector_type(4)));
typedef float f32x16 __attribute__((ext_vector_type(16)));
typedef unsigned u32x2 __attribute__((ext_vector_type(2)));
typedef unsigned u32x4 __attribute__((ext_vector_type(4)));
typedef __bf16 bf2_t __attribute__((ext_vector_type(2)));
typedef _Float16 h2_t __attribute__((ext_vector_type(2)));
typedef _Float16 h8_t __attribute__((ext_vector_type(8)));
#define LAS __attribute__((address_space(3)))
#define DEV __device__ __forceinline__
#define SCHED() __builtin_amdgcn_sched_barrier(0)
#define CBAR() asm volatile("" ::: "memory")
#define WAITV(n) asm volatile("s_waitcnt vmcnt(%0)" ::"i"(n) : "memory")
#define WAITL0() asm volatile("s_waitcnt lgkmcnt(0)" ::: "memory")
#define HWBAR() __builtin_amdgcn_s_barrier()

constexpr int SEQ = 2048, DM = 640, NST = 192, FF = 1536, FH = 768;

constexpr u32 WS_EPOCH = 0;
constexpr u32 WS_FZ2 = 4096;
constexpr u32 WS_FA = WS_FZ2 + 256 * 256;
constexpr u32 WS_FUT = WS_FA + 64 * 256;
constexpr u32 WS_FH = WS_FUT + 64 * 256;
constexpr u32 WS_STATS = WS_FH + 4 * 256 * 256;
constexpr u32 WS_Z2 = 1u << 20;
constexpr u32 WS_H = WS_Z2 + SEQ * FH * 2;
constexpr u32 WS_A = WS_H + SEQ * FH * 2;
constexpr u32 WS_UT = WS_A + SEQ * DM * 2;
constexpr u32 WS_END = WS_UT + SEQ * DM * 2;
static_assert(WS_STATS + 32 * 6 * 1024 <= WS_Z2, "ws map");

constexpr u32 L_TOTAL = 163840, L_FLG = L_TOTAL - 256, L_GATE = L_FLG - 256;
constexpr u32 L_X = 0;
constexpr u32 L_RING = 81920, CHUNK = 16384;
constexpr u32 L_GW = 155648;
constexpr u32 L_PART = L_GW + 1280;
constexpr u32 L_RS = L_PART + 3072;
constexpr u32 L_GP = L_RS + 512;
static_assert(L_GP + 1024 <= L_GATE, "lds");
constexpr u32 L_Z1 = 65536;
constexpr u32 L_RED = 0;
constexpr u32 L_W0 = 65536, L_W1 = 114688, L_W1B = 0, L_W2 = 16384, L_W3 = L_W0;
constexpr u32 L_AT = L_W1;
constexpr u32 SC_SZ = 8704, L_T = 8 * SC_SZ, T_SZ = 4608, U_SZ = 4224, L_UA = L_T + 10 * T_SZ;
static_assert(L_UA % 128 == 0 && L_UA + 10 * U_SZ <= L_GATE && SC_SZ % 128 == 0, "lds");
DEV u32 ub_base(int k) { return k < 8 ? k * SC_SZ + 4288 : (k - 8) * SC_SZ + 64; }
enum { F_H3 = 0, F_H3F = 1, F_C0 = 2, F_C1 = 3, F_L2 = 4, F_L3 = 5, F_AT = 6, F_AR = 7, F_XT = 8, F_ACNT = 9, F_P1 = 10, F_P2 = 11, F_P3 = 12, F_C2 = 13, F_UC = 14, F_PC = 15, F_EX = 16 };
#define SPRIO 2
#define TLBW 2
#define ROT4EXPR 0
#define ROTEXPR (ti + 3 * tj) % 10
#define CODE_LINES 330u

#define XST(k)
#define STAMP(k)
#define TSARG
#define TSPASS
#define TSFLUSH()

struct Params {
  const u16 *u, *Are, *Aim, *Bre, *Bim, *Cre, *Cim, *logdt, *dskip, *w_in, *w_sp, *b_sp, *w_out, *gate_w;
  u16* out;
  char* ws;
};

typedef const __attribute__((address_space(4))) Params* KP;
DEV KP kargs() {
  KP p = (KP)__builtin_amdgcn_kernarg_segment_ptr();
  asm volatile("" : "+s"(p));
  return p;
}
DEV float bf2f(u32 v) { return __uint_as_float(v << 16); }
DEV float bfhi(u32 v) { return __uint_as_float(v & 0xffff0000u); }
DEV u32 pk2(float a, float b) { return __builtin_bit_cast(u32, __builtin_convertvector(f32x2{a, b}, bf2_t)); }
DEV u32x4 mk_srd(const void* p, u32 bytes) { return u32x4{(u32)(size_t)p, (u32)((size_t)p >> 32) & 0xffffu, bytes, 0x00020000u}; }
DEV __amdgpu_buffer_rsrc_t mk_rsrc(const void* p, u32 bytes) { return __builtin_amdgcn_make_buffer_rsrc((void*)p, 0, bytes, 0x00020000); }
DEV void dma16(u32x4 srd, u32 voff, u32 soff, u32 dst) {
  asm volatile("s_mov_b32 m0, %2\n\ts_nop 0\n\tbuffer_load_dwordx4 %0, %1, %3 offen lds" ::"v"(voff), "s"(srd), "s"(dst), "s"(soff) : "memory");
}
DEV void m0_set(u32 dst) { asm volatile("s_mov_b32 m0, %0" ::"s"(dst) : "memory"); }
template <int OFF> DEV void dma16o(u32x4 srd, u32 voff, u32 soff) {
  asm volatile("buffer_load_dwordx4 %0, %1, %2 offen offset:%3 lds" ::"v"(voff), "s"(srd), "s"(soff), "i"(OFF) : "memory");
}
DEV void bload(u32x4& d, u32x4 srd, u32 voff, u32 soff) {
  asm volatile("buffer_load_dwordx4 %0, %1, %2, %3 offen" : "=v"(d) : "v"(voff), "s"(srd), "s"(soff) : "memory");
}
DEV void bload_sc1(u32x4& d, u32x4 srd, u32 voff, u32 soff) {
  asm volatile("buffer_load_dwordx4 %0, %1, %2, %3 offen sc1" : "=v"(d) : "v"(voff), "s"(srd), "s"(soff) : "memory");
}
#define WAITV_PIN(n, x) asm volatile("s_waitcnt vmcnt(%1)" : "+v"(x) : "i"(n) : "memory")
template <class T> DEV LAS T* lp(u32 base, u32 off = 0) { return (LAS T*)((LAS char*)(size_t)base + off); }
DEV u32 smask(bool c) { u32 m = __builtin_amdgcn_readfirstlane(c ? 1 : 0); asm volatile("s_nop 0" : "+s"(m)); return m; }
DEV u32 opq(u32 x) { asm volatile("" : "+v"(x)); return x; }
DEV bf16x8 lds128(u32 base, u32 off = 0) { return *lp<bf16x8>(base, off); }
DEV s16x4 ldstr(u32 base, u32 off = 0) { return __builtin_amdgcn_ds_read_tr16_b64_v4i16(lp<s16x4>(base, off)); }
DEV bf16x8 cat8(s16x4 a, s16x4 b) { return __builtin_shufflevector(a, b, 0, 1, 2, 3, 4, 5, 6, 7); }
DEV bf16x8 as8(u32x4 v) { return __builtin_bit_cast(bf16x8, v); }

DEV u32 faddr(int k) { return L_FLG + 4 * k; }
DEV void flag_st(u32 addr, u32 v) { asm volatile("ds_write_b32 %0, %1" ::"v"(addr), "v"(v) : "memory"); }
DEV void flag_inc(u32 addr) {
  asm volatile("s_mov_b64 exec, 1\n\tds_add_u32 %0, %1\n\ts_mov_b64 exec, -1" ::"v"(addr), "v"(1u) : "memory");
}
DEV void flag_pre(u32& pre, u32 addr) { asm volatile("ds_read_b32 %0, %1" : "=v"(pre) : "v"(addr) : "memory"); }
template <int SLEEP = 1> DEV void flag_wait_pre(u32 pre, u32 addr, u32 want) {
  u32 t, cnt, v;
  asm volatile("s_waitcnt lgkmcnt(0)\n\t"
               "v_readfirstlane_b32 %0, %6\n\t"
               "s_cmp_ge_u32 %0, %4\n\t"
               "s_cbranch_scc1 .Lfd%=\n\t"
               "s_mov_b32 %1, 0x20000\n"
               ".Lfw%=:\n\t"
               "s_sleep %5\n\t"
               "ds_read_b32 %2, %3\n\t"
               "s_sub_u32 %1, %1, 1\n\t"
               "s_cbranch_scc1 .Lfe%=\n\t"
               "s_waitcnt lgkmcnt(0)\n\t"
               "v_readfirstlane_b32 %0, %2\n\t"
               "s_cmp_lt_u32 %0, %4\n\t"
               "s_cbranch_scc1 .Lfw%=\n"
               ".Lfe%=:\n\t"
               "s_waitcnt lgkmcnt(0)\n"
               ".Lfd%=:"
               : "=&s"(t), "=&s"(cnt), "=&v"(v) : "v"(addr), "s"(want), "i"(SLEEP), "v"(pre) : "memory", "scc");
}
template <int SLEEP = 1> DEV void flag_wait(u32 addr, u32 want) {
  u32 pre;
  flag_pre(pre, addr);
  flag_wait_pre<SLEEP>(pre, addr, want);
}
DEV void gflag_st(u32x4 srd, u32 off, u32 tag, u32 mask = 1) {
  asm volatile("s_mov_b32 exec_lo, %3\n\ts_mov_b32 exec_hi, 0\n\tbuffer_store_dword %0, off, %1, %2 sc1\n\ts_mov_b64 exec, -1" ::"v"(tag), "s"(srd), "s"(off), "s"(mask) : "memory");
}
DEV void gflag_st_last(u32 addr, u32 last, u32x4 srd, u32 off, u32 tag) {
  u32 old, t;
  u64 m;
  asm volatile("s_mov_b64 exec, 1\n\t"
               "ds_add_rtn_u32 %0, %3, %4\n\t"
               "s_waitcnt lgkmcnt(0)\n\t"
               "v_readfirstlane_b32 %1, %0\n\t"
               "s_cmp_eq_u32 %1, %5\n\t"
               "s_cselect_b64 %2, 1, 0\n\t"
               "s_mov_b64 exec, %2\n\t"
               "buffer_store_dword %8, off, %6, %7 sc1\n\t"
               "s_mov_b64 exec, -1"
               : "=&v"(old), "=&s"(t), "=&s"(m) : "v"(addr), "v"(1u), "s"(last), "s"(srd), "s"(off), "v"(tag) : "memory", "scc");
}
DEV void gflag_wait(u32x4 srd, u32 voff, u32 tag) {
  u32 v, cnt;
  asm volatile("s_mov_b32 %1, 0x4000\n"
               ".Lgw%=:\n\t"
               "buffer_load_dword %0, %2, %3, 0 offen sc1\n\t"
               "s_waitcnt vmcnt(0)\n\t"
               "v_cmp_ne_u32 vcc, %4, %0\n\t"
               "s_cbranch_vccz .Lgd%=\n\t"
               "s_sleep 2\n\t"
               "s_sub_u32 %1, %1, 1\n\t"
               "s_cbranch_scc0 .Lgw%=\n"
               ".Lgd%=:"
               : "=&v"(v), "=&s"(cnt) : "v"(voff), "s"(srd), "s"(tag) : "memory", "scc", "vcc");
}

DEV void gflag_check(u32 v0, u32x4 srd, u32 voff, u32 tag) {
  u32 v, cnt;
  asm volatile("v_cmp_ne_u32 vcc, %4, %5\n\t"
               "s_cbranch_vccz .Lgd%=\n\t"
               "s_mov_b32 %1, 0x4000\n"
               ".Lgw%=:\n\t"
               "buffer_load_dword %0, %2, %3, 0 offen sc1 ; half 1\n\t"
               "s_waitcnt vmcnt(0) ; half 1\n\t"
               "v_cmp_ne_u32 vcc, %4, %0\n\t"
               "s_cbranch_vccz .Lgd%=\n\t"
               "s_sleep 2\n\t"
               "s_sub_u32 %1, %1, 1\n\t"
               "s_cbranch_scc0 .Lgw%=\n"
               ".Lgd%=:"
               : "=&v"(v), "=&s"(cnt) : "v"(voff), "s"(srd), "s"(tag), "v"(v0) : "memory", "scc", "vcc");
}

DEV float gelu_f(float a) {
  const float k0 = -2.3022081999f, k1 = -0.1029432397f;
  const float e = __builtin_amdgcn_exp2f(a * __builtin_fmaf(a * a, k1, k0));
  return a * __builtin_amdgcn_rcpf(1.0f + e);
}
template <int CTRL> DEV float dppf(float x) { return __builtin_bit_cast(float, __builtin_amdgcn_update_dpp(0, __builtin_bit_cast(int, x), CTRL, 0xf, 0xf, false)); }
#define MFMA16(a, b, c) __builtin_amdgcn_mfma_f32_16x16x32_bf16(a, b, c, 0, 0, 0)
#define MFMA32H(a, b, c) __builtin_amdgcn_mfma_f32_32x32x16_f16(a, b, c, 0, 0, 0)

DEV f32x4 rowsum16(const float (&v)[16]) {
  float a[8], o0, o1, o2, o3;
#define RS1(d, x, y) "v_add_f32_dpp " d ", " x ", " x " row_ror:8 row_mask:0xf bank_mask:0x3\n\tv_add_f32_dpp " d ", " y ", " y " row_ror:8 row_mask:0xf bank_mask:0xc\n\t"
  asm volatile("s_nop 1\n\t" RS1("%0", "%8", "%16") RS1("%1", "%9", "%17") RS1("%2", "%10", "%18") RS1("%3", "%11", "%19")
               RS1("%4", "%12", "%20") RS1("%5", "%13", "%21") RS1("%6", "%14", "%22") RS1("%7", "%15", "%23")
               : "=&v"(a[0]), "=&v"(a[1]), "=&v"(a[2]), "=&v"(a[3]), "=&v"(a[4]), "=&v"(a[5]), "=&v"(a[6]), "=&v"(a[7])
               : "v"(v[0]), "v"(v[1]), "v"(v[2]), "v"(v[3]), "v"(v[4]), "v"(v[5]), "v"(v[6]), "v"(v[7]),
                 "v"(v[8]), "v"(v[9]), "v"(v[10]), "v"(v[11]), "v"(v[12]), "v"(v[13]), "v"(v[14]), "v"(v[15]));
#define RS2(d, x, y) "v_add_f32_dpp " d ", " x ", " x " row_half_mirror row_mask:0xf bank_mask:0x5\n\tv_add_f32_dpp " d ", " y ", " y " row_half_mirror row_mask:0xf bank_mask:0xa\n\t"
#define RS3(qp) "v_add_f32_dpp %0, %0, %0 quad_perm:" qp " row_mask:0xf bank_mask:0xf\n\tv_add_f32_dpp %1, %1, %1 quad_perm:" qp " row_mask:0xf bank_mask:0xf\n\t" \
                "v_add_f32_dpp %2, %2, %2 quad_perm:" qp " row_mask:0xf bank_mask:0xf\n\tv_add_f32_dpp %3, %3, %3 quad_perm:" qp " row_mask:0xf bank_mask:0xf\n\t"
  asm volatile("s_nop 1\n\t" RS2("%0", "%4", "%8") RS2("%1", "%5", "%9") RS2("%2", "%6", "%10") RS2("%3", "%7", "%11") RS3("[1,0,3,2]") RS3("[2,3,0,1]")
               : "=&v"(o0), "=&v"(o1), "=&v"(o2), "=&v"(o3)
               : "v"(a[0]), "v"(a[1]), "v"(a[2]), "v"(a[3]), "v"(a[4]), "v"(a[5]), "v"(a[6]), "v"(a[7]));
  return f32x4{o0, o1, o2, o3};
}
DEV void mfma16_ip(f32x4& c, bf16x8 a, bf16x8 b) { asm volatile("v_mfma_f32_16x16x32_bf16 %0, %1, %2, %0" : "+v"(c) : "v"(a), "v"(b)); }
DEV void mfma16_z(f32x4& c, bf16x8 a, bf16x8 b) { asm volatile("v_mfma_f32_16x16x32_bf16 %0, %1, %2, 0" : "=&v"(c) : "v"(a), "v"(b)); }
DEV h2_t hmul(h2_t x, h2_t e) { return __builtin_elementwise_fma(h2_t{x.y, x.y}, h2_t{-e.y, e.x}, h2_t{x.x, x.x} * e); }
DEV h2_t toh(f32x2 v) { return __builtin_convertvector(v, h2_t); }
DEV f32x2 csq(f32x2 e) {
  float t, x, y;
  asm("v_mul_f32 %0, %3, %3\n\tv_add_f32 %1, %2, %2\n\tv_fma_f32 %0, %2, %2, -%0\n\tv_mul_f32 %1, %1, %3" : "=&v"(x), "=&v"(y) : "v"(e.x), "v"(e.y));
  (void)t;
  return f32x2{x, y};
}
DEV f32x2 cexp2(float ar, float ai, float l) {
  const float m = __builtin_amdgcn_exp2f(ar * l), th = __builtin_amdgcn_fractf(ai * l);
  return f32x2{__uint_as_float(opq(__float_as_uint(m * __builtin_amdgcn_cosf(th)))), m * __builtin_amdgcn_sinf(th)};
}

struct TUnit { u32 t[4][4], o[4][4]; };
#define TSTEP_ASM(qp) \
  "s_mov_b32 vcc_lo, %24\n\ts_mov_b32 vcc_hi, %24\n\t" \
  "v_cndmask_b32_dpp %0, %16, %8, vcc quad_perm:" qp " row_mask:0xf bank_mask:0xf\n\t" \
  "v_cndmask_b32_dpp %1, %17, %9, vcc quad_perm:" qp " row_mask:0xf bank_mask:0xf\n\t" \
  "v_cndmask_b32_dpp %2, %18, %10, vcc quad_perm:" qp " row_mask:0xf bank_mask:0xf\n\t" \
  "v_cndmask_b32_dpp %3, %19, %11, vcc quad_perm:" qp " row_mask:0xf bank_mask:0xf\n\t" \
  "v_cndmask_b32_dpp %4, %20, %12, vcc quad_perm:" qp " row_mask:0xf bank_mask:0xf\n\t" \
  "v_cndmask_b32_dpp %5, %21, %13, vcc quad_perm:" qp " row_mask:0xf bank_mask:0xf\n\t" \
  "v_cndmask_b32_dpp %6, %22, %14, vcc quad_perm:" qp " row_mask:0xf bank_mask:0xf\n\t" \
  "v_cndmask_b32_dpp %7, %23, %15, vcc quad_perm:" qp " row_mask:0xf bank_mask:0xf"
#define TSTEP_OPS(o, g0, g1, s, x) \
  : "=&v"(o[g0][0]), "=&v"(o[g0][1]), "=&v"(o[g0][2]), "=&v"(o[g0][3]), "=&v"(o[g1][0]), "=&v"(o[g1][1]), "=&v"(o[g1][2]), "=&v"(o[g1][3]) \
  : "v"(s[g0][0]), "v"(s[g0][1]), "v"(s[g0][2]), "v"(s[g0][3]), "v"(s[g1][0]), "v"(s[g1][1]), "v"(s[g1][2]), "v"(s[g1][3]), \
    "v"(s[(g0) xor (x)][0]), "v"(s[(g0) xor (x)][1]), "v"(s[(g0) xor (x)][2]), "v"(s[(g0) xor (x)][3]), "v"(s[(g1) xor (x)][0]), "v"(s[(g1) xor (x)][1]), "v"(s[(g1) xor (x)][2]), "v"(s[(g1) xor (x)][3])
DEV void tstep(TUnit& U, const u32x4 (&r)[4], int k) {
  if (k == 0) asm volatile(TSTEP_ASM("[1,0,3,2]") TSTEP_OPS(U.t, 0, 2, r, 1), "i"(0x55555555u) : "vcc");
  if (k == 1) asm volatile(TSTEP_ASM("[1,0,3,2]") TSTEP_OPS(U.t, 1, 3, r, 1), "i"(0xAAAAAAAAu) : "vcc");
  if (k == 2) asm volatile(TSTEP_ASM("[2,3,0,1]") TSTEP_OPS(U.o, 0, 1, U.t, 2), "i"(0x33333333u) : "vcc");
  if (k == 3) asm volatile(TSTEP_ASM("[2,3,0,1]") "\n\ts_nop 1" TSTEP_OPS(U.o, 2, 3, U.t, 2), "i"(0xCCCCCCCCu) : "vcc");
}
DEV u32x4 tout(const TUnit& U, int g) { return u32x4{U.o[g][0], U.o[g][1], U.o[g][2], U.o[g][3]}; }

template <int V> struct IC { static constexpr int value = V; };
template <int I, int N, class F> DEV void sfor(F&& f) {
  if constexpr (I < N) { f(IC<I>{}); sfor<I + 1, N>(f); }
}
#define BARRIER() do { WAITL0(); HWBAR(); SCHED(); } while (0)

template <class F, class G>
DEV void kgen(f32x16& kacc, const u32x4 (&praw)[3], const u32 (&px)[6], float ldt, float ldtx, u32 sr, u32 T, int lane, F&& shared_done, G&& early) {
  const float dt0 = bf2f(pk2(expf(ldt), 0.f) & 0xffffu), dtx = bf2f(pk2(expf(ldtx), 0.f) & 0xffffu);
  const u32 rdp = opq(sr + (u32)((lane & 31) * 272 + (lane >> 5) * 16));
#pragma unroll
  for (int k = 0; k < 3; ++k) *lp<u32x4>(T + 1024 + lane * 16, k * 1024) = praw[k];
  CBAR();
  const u32 prd = opq(T + 1024 + lane * 2);
  m0_set(sr);
  auto put = [&](int row, h2_t v) { asm volatile("ds_write_addtid_b32 %0 offset:%1" ::"v"(v), "i"(row * 272) : "memory"); };
  kacc = f32x16{};
  float ar[4], ai[4], wr[4], wi[4], mx[2] = {0.f, 0.f}, wsc[2], kinv[2];
#pragma unroll
  for (int ps = 0; ps < 4; ++ps) {
    u32 pw[6];
#pragma unroll
    for (int k = 0; k < 6; ++k) pw[k] = ps ? (u32)*lp<u16>(prd, (k >> 1) * 1024 + (k & 1) * 384 + (ps - 1) * 128) : (lane < 48 ? px[k] : 0u);
    const float dt = ps ? dt0 : dtx;
    ar[ps] = (dt * bf2f(pw[0])) * 1.4426950409f;
    ai[ps] = (dt * bf2f(pw[1])) * 0.15915494309f;
    const float br = dt * bf2f(pw[2]), bi = dt * bf2f(pw[3]), cr = bf2f(pw[4]), ci = bf2f(pw[5]);
    wr[ps] = __uint_as_float(opq(__float_as_uint(__builtin_fmaf(cr, br, -ci * bi))));
    wi[ps] = -__builtin_fmaf(cr, bi, ci * br);
    mx[ps == 0] = fmaxf(mx[ps == 0], fmaxf(fabsf(wr[ps]), fabsf(wi[ps])));
  }
#pragma unroll
  for (int k = 0; k < 2; ++k) {
    float m = mx[k];
    m = fmaxf(m, dppf<0x128>(m)); m = fmaxf(m, dppf<0x124>(m)); m = fmaxf(m, dppf<0x122>(m)); m = fmaxf(m, dppf<0x121>(m));
    const u32 mb = __float_as_uint(m);
    const u32 ex = min(max(max(max((u32)__builtin_amdgcn_readlane(mb, 0), (u32)__builtin_amdgcn_readlane(mb, 16)),
                               max((u32)__builtin_amdgcn_readlane(mb, 32), (u32)__builtin_amdgcn_readlane(mb, 48))) >> 23, 1u), 252u);
    wsc[k] = __uint_as_float((253u - ex) << 23);
    kinv[k] = __uint_as_float((ex + 1u) << 23);
  }
  h2_t gw[4];
#pragma unroll
  for (int ps = 0; ps < 4; ++ps) gw[ps] = toh(f32x2{__uint_as_float(opq(__float_as_uint(wr[ps] * wsc[ps == 0]))), wi[ps] * wsc[ps == 0]});
  h2_t c1, c2, c4, c256, c512, c1024, g0, qv[32];
  auto setup = [&]() {
    const f32x2 e1 = cexp2(ar[0], -ai[0], 1.f), e8 = cexp2(ar[0], ai[0], 8.f), e256 = cexp2(ar[0], -ai[0], 256.f);
    const f32x2 e2 = csq(e1), e16 = csq(e8), e512 = csq(e256), e4 = csq(e2), e32 = csq(e16), e1024 = csq(e512), e64 = csq(e32), e128 = csq(e64);
    c1 = toh(e1); c2 = toh(e2); c4 = toh(e4); c256 = toh(e256); c512 = toh(e512); c1024 = toh(e1024);
    qv[1] = toh(e8); qv[2] = toh(e16); qv[4] = toh(e32); qv[8] = toh(e64); qv[16] = toh(e128);
    g0 = gw[0];
#pragma unroll
    for (int k = 0; k < 3; ++k) { ar[k] = ar[k + 1]; ai[k] = ai[k + 1]; gw[k] = gw[k + 1]; }
  };
  qv[0] = h2_t{(_Float16)1.f, (_Float16)0.f};
  setup();
  SCHED();
#pragma unroll
  for (int ps = 0; ps < 4; ++ps) {
    early(ps);
    SCHED();
    {
      const h2_t p1 = hmul(g0, c1), p2 = hmul(g0, c2), p4 = hmul(g0, c4), g1 = hmul(g0, c256), g2 = hmul(g0, c512), g4 = hmul(g0, c1024);
      const h2_t p3 = hmul(p1, c2), p258 = hmul(g1, c2), g3 = hmul(g1, c512), p5 = hmul(p1, c4);
      SCHED();
      put(0, g0); put(1, p1); put(2, p2); put(4, p4); put(8, g1); put(12, g2); put(16, g4);
      SCHED();
      const h2_t p6 = hmul(p2, c4), p260 = hmul(g1, c4), p516 = hmul(g2, c4), g5 = hmul(g1, c1024), g6 = hmul(g2, c1024), g8 = hmul(g4, c1024);
      const h2_t p7 = hmul(p3, c4), p262 = hmul(p258, c4), p772 = hmul(g3, c4), g7 = hmul(g3, c1024);
      SCHED();
      put(3, p3); put(9, p258); put(14, g3); put(5, p5); put(6, p6); put(10, p260); put(13, p516);
      SCHED();
      qv[3] = hmul(qv[1], qv[2]); qv[5] = hmul(qv[1], qv[4]); qv[6] = hmul(qv[2], qv[4]); qv[24] = hmul(qv[8], qv[16]);
      qv[9] = hmul(qv[1], qv[8]); qv[10] = hmul(qv[2], qv[8]); qv[12] = hmul(qv[4], qv[8]); qv[7] = hmul(qv[3], qv[4]);
      SCHED();
      put(17, g5); put(18, g6); put(20, g8); put(7, p7); put(11, p262); put(15, p772); put(19, g7);
    }
    h8_t pf[8], qf[8];
#pragma unroll
    for (int kk = 0; kk < 8; ++kk) pf[kk] = *lp<h8_t>(rdp, kk * 32);
    CBAR();
    SCHED();
    qv[11] = hmul(qv[3], qv[8]); qv[13] = hmul(qv[5], qv[8]); qv[14] = hmul(qv[6], qv[8]); qv[15] = hmul(qv[7], qv[8]);
    SCHED();
#pragma unroll
    for (int j = 0; j < 8; ++j) put(j, qv[j]);
    SCHED();
#pragma unroll
    for (int j = 1; j < 8; ++j) qv[16 + j] = hmul(qv[j], qv[16]);
    SCHED();
#pragma unroll
    for (int j = 8; j < 16; ++j) put(j, qv[j]);
    SCHED();
#pragma unroll
    for (int j = 1; j < 8; ++j) qv[24 + j] = hmul(qv[j], qv[24]);
    SCHED();
#pragma unroll
    for (int j = 16; j < 32; ++j) put(j, qv[j]);
#pragma unroll
    for (int kk = 0; kk < 8; ++kk) qf[kk] = *lp<h8_t>(rdp, kk * 32);
    CBAR();
    SCHED();
    if (ps == 1) {
#pragma unroll
      for (int r = 0; r < 12; ++r) kacc[r] = __uint_as_float(opq(__float_as_uint(kacc[r] * kinv[1])));
      shared_done(kacc);
      kacc = f32x16{};
    }
    SCHED();
    if (ps < 3) setup();
#pragma unroll
    for (int kk = 0; kk < 8; ++kk) kacc = MFMA32H(pf[kk], qf[kk], kacc);
    __builtin_amdgcn_sched_group_barrier(0x402, 20, 0);
#pragma unroll
    for (int kk = 0; kk < 8; ++kk) { __builtin_amdgcn_sched_group_barrier(0x8, 1, 0); __builtin_amdgcn_sched_group_barrier(0x402, 5, 0); }
    SCHED();
  }
#pragma unroll
  for (int r = 0; r < 12; ++r) kacc[r] = __uint_as_float(opq(__float_as_uint(kacc[r] * kinv[0])));
}
DEV void expand(f32x16 kacc, float dsk, u32 CB, u32 T, int lane) {
  const int bq = lane & 31, hh = lane >> 5;
  kacc[0] += __uint_as_float(opq(lane == 0 ? __float_as_uint(dsk) : 0u));
  *lp<u32x2>(T + lane * 8) = u32x2{0u, 0u};
  *lp<u32x2>(T + 512 + (u32)(bq * 16 + hh * 8)) = u32x2{pk2(kacc[0], kacc[1]), pk2(kacc[2], kacc[3])};
  *lp<u32>(CB + (u32)(hh ? 256 + 4 * bq : 8 * bq)) = pk2(kacc[4], kacc[5]);
  *lp<u32>(CB + (u32)(hh ? 384 + 4 * bq : 8 * bq + 4)) = pk2(kacc[6], kacc[7]);
#pragma unroll
  for (int r = 0; r < 4; ++r) *lp<u16>(CB + (u32)(512 + 64 * (4 * hh + r) + 2 * bq)) = (u16)pk2(kacc[8 + r], 0.f);
  CBAR();
  {
    const u32x2 v = *lp<u32x2>(CB + (u32)(8 * bq));
    const u32 n4 = *lp<u16>(CB + (u32)(8 * bq + 8));
    const u32x4 o = {__builtin_amdgcn_perm(v.x, v.x, 0x01000100u), __builtin_amdgcn_perm(v.y, v.x, 0x05040302u),
                     __builtin_amdgcn_perm(v.y, v.y, 0x01000100u), __builtin_amdgcn_perm(n4, v.y, 0x05040302u)};
    *lp<u32x4>(T + 1024 + (u32)(bq * 16)) = o;
  }
  {
    const u32 v = *lp<u32>(CB + (u32)(256 + 4 * lane));
    const u32 n2 = *lp<u16>(CB + (u32)(256 + 4 * lane + 4));
    *lp<u32x4>(T + 1536 + (u32)(lane * 16)) = u32x4{__builtin_amdgcn_perm(v, v, 0x01000100u), v, __builtin_amdgcn_perm(v, v, 0x03020302u), n2 * 0x10001u};
  }
#pragma unroll
  for (int g = 0; g < 2; ++g) {
    const u32 v0 = *lp<u16>(CB + (u32)(512 + 2 * (g * 64 + lane))), v1 = *lp<u16>(CB + (u32)(514 + 2 * (g * 64 + lane)));
    *lp<u32x4>(T + 2560 + (u32)((g * 64 + lane) * 16)) = u32x4{v0 * 0x10001u, v0 * 0x10001u, ((lane & 1) ? v1 : v0) | (v1 << 16), v1 * 0x10001u};
  }
  CBAR();
}
DEV void prm_load(KP kp, u32x4 (&praw)[3], u32& ldt, u32& dsk, int chn, int lane) {
  const int l24 = lane < 24 ? lane : (lane < 48 ? lane - 24 : 0);
  const size_t po = (size_t)chn * NST + l24 * 8;
  praw[0] = *(const u32x4*)((lane < 24 ? kp->Are : kp->Aim) + po);
  praw[1] = *(const u32x4*)((lane < 24 ? kp->Bre : kp->Bim) + po);
  praw[2] = *(const u32x4*)((lane < 24 ? kp->Cre : kp->Cim) + po);
  ldt = kp->logdt[chn];
  dsk = kp->dskip[chn];
}
DEV void u_fetch(char* ws, int k, int chn, int lane) {
  const __amdgpu_buffer_rsrc_t r_ws = mk_rsrc(ws, WS_END);
  const u32 ra = opq(L_UA + k * U_SZ + lane * 16), wb = opq(ub_base(k) + lane * 16);
  u32x4 d[4];
  u32 n[4];
#pragma unroll
  for (int g = 0; g < 4; ++g) d[g] = __builtin_amdgcn_raw_buffer_load_b128(r_ws, WS_UT + (u32)(chn * 4096 + lane * 16), g * 1024, 16);
  *lp<u32>((lane < 32 ? L_UA + k * U_SZ : ub_base(k)) + 4096 + (lane & 15) * 4) = 0u;
#pragma unroll
  for (int g = 0; g < 4; ++g) *lp<u32x4>(ra, g * 1024) = d[g];
  CBAR();
#pragma unroll
  for (int g = 0; g < 4; ++g) n[g] = *lp<u32>(ra, g * 1024 + 16);
#pragma unroll
  for (int g = 0; g < 4; ++g)
    *lp<u32x4>(wb, g * 1024) = u32x4{__builtin_amdgcn_alignbit(d[g].y, d[g].x, 16), __builtin_amdgcn_alignbit(d[g].z, d[g].y, 16),
                                     __builtin_amdgcn_alignbit(d[g].w, d[g].z, 16), __builtin_amdgcn_alignbit(n[g], d[g].w, 16)};
  CBAR();
}
template <int TM> DEV void conv(char* ws, int k, int chn, u32 tag, int sidx, int lane) {
  constexpr int NT = TM >= 64 ? 8 : 6;
  const int c16 = lane & 15, q = lane >> 4;
  u32 ab[4], bb = opq(L_T + k * T_SZ + (u32)(32 * c16 + 16 * q + 480));
#pragma unroll
  for (int h = 0; h < 4; ++h) ab[h] = opq(((c16 & 1) ? L_UA + k * U_SZ : ub_base(k)) + (u32)(3548 - 1024 * h - 4 * (c16 >> 1) + 16 * q));
  auto ldA = [&](int p) {
    const u32 o = (p & 1) ? 0 : 512;
    return u32x4{*lp<u32>(ab[p >> 1], o), *lp<u32>(ab[p >> 1], o + 4), *lp<u32>(ab[p >> 1], o + 8), *lp<u32>(ab[p >> 1], o + 12)};
  };
  f32x4 y[8] = {};
#pragma unroll
  for (int c = 0; c < 8; ++c) asm volatile("" : "+v"(y[c]));
  u32x4 fa[8];
  bf16x8 fb[8];
#pragma unroll
  for (int p = NT - 1; p >= 0; --p) { fa[p] = ldA(p); fb[NT - 1 - p] = lds128(bb, 512 * (NT - 1 - p)); }
#pragma nounroll
  for (int r = 0; r < 8; ++r) {
    bb -= 64;
#pragma unroll
    for (int h = 0; h < 4; ++h) ab[h] -= 64;
    SCHED();
#pragma unroll
    for (int p = NT - 1; p >= 0; --p) {
#pragma unroll
      for (int m = 0; m + p < NT; ++m) {
        if ((TM >> (p + m)) & 1) mfma16_ip(y[p + m], as8(fa[p]), fb[m]);
        if (p == 0) { SCHED(); fb[m] = lds128(bb, 512 * m); SCHED(); }
      }
      SCHED();
      fa[p] = ldA(p);
      SCHED();
    }
  }
  asm volatile("s_nop 7" : "+v"(y[0]), "+v"(y[1]), "+v"(y[2]), "+v"(y[3]), "+v"(y[4]), "+v"(y[5]), "+v"(y[6]), "+v"(y[7]));
  const __amdgpu_buffer_rsrc_t r_ws = mk_rsrc(ws, WS_END);
  const u32 ao = WS_A + (u32)(chn * 4096 + (q & 1) * 512 + c16 * 32 + (q >> 1) * 16);
#pragma unroll
  for (int c = 0; c < 8; c += 2) {
    if ((TM >> c) & 1) {
      const auto s0 = __builtin_amdgcn_permlane16_swap(pk2(y[c][0], y[c][1]), pk2(y[c + 1][0], y[c + 1][1]), false, false);
      const auto s1 = __builtin_amdgcn_permlane16_swap(pk2(y[c][2], y[c][3]), pk2(y[c + 1][2], y[c + 1][3]), false, false);
      __builtin_amdgcn_raw_buffer_store_b128(u32x4{s0[0], s1[0], s0[1], s1[1]}, r_ws, ao + c * 512, 0, 16);
    }
  }
}
DEV void ssm_wg(char* ws, u32 ep_raw, u32 pf_raw, int sidx, int w, int lane TSARG) {
  const int c0 = 10 * sidx, cx = c0 + 8 + (w >> 2), c16 = lane & 15, q = lane >> 4;
  const KP kp = kargs();
  const u32x4 s_ws = mk_srd(ws, WS_END);
  const u32 sc = L_UA + (w & 3) * 10240;
  const int tch = 320 * (sidx & 1) + 80 * (w & 3);
  u32x4 praw[3];
  u32 ldt, dsk, px[6];
  prm_load(kp, praw, ldt, dsk, c0 + w, lane);
  {
    const int o = cx * NST + 48 * (w & 3) + min(lane, 47);
    px[0] = kp->Are[o]; px[1] = kp->Aim[o]; px[2] = kp->Bre[o]; px[3] = kp->Bim[o]; px[4] = kp->Cre[o]; px[5] = kp->Cim[o];
  }
  const u32 ldtx = kp->logdt[cx], dskx = kp->dskip[c0 + 8 + (w & 1)];
  if (w >= 4) {
    const u32x4 s_x = mk_srd(kp->u, SEQ * DM * 2);
#pragma unroll
    for (int u = 0; u < 5; ++u) {
      const int off = u * 1024 + lane * 16;
      const u32 vo = (u32)((off / 160) * 1280 + (off % 160) + (sidx >> 1) * 64 * 1280 + tch * 2);
      dma16(s_x, vo, 0, sc + u * 1024);
      dma16(s_x, vo, 32 * 1280, sc + 5120 + u * 1024);
    }
  }
  BARRIER();
  u32 tag;
  asm volatile("s_waitcnt vmcnt(0)\n\tv_readfirstlane_b32 %0, %1\n\ts_nop 1" : "=s"(tag) : "v"(ep_raw), "v"(pf_raw) : "memory");
  tag += 1u;
  if (w >= 4) {
    __builtin_amdgcn_s_setprio(3);
    const u32 hr = opq(sc + (u32)((8 * q + 3 - ((lane >> 2) & 3)) * 160 + 8 * (lane & 3)));
    bf16x8 tv[2][5];
#pragma unroll
    for (int g = 0; g < 2; ++g)
#pragma unroll
      for (int e = 0; e < 5; ++e) tv[g][e] = cat8(ldstr(hr, g * 5120 + 640 + 32 * e), ldstr(hr, g * 5120 + 32 * e));
    CBAR();
#pragma unroll
    for (int g = 0; g < 2; ++g) {
      const u32 tw = opq(sc + (u32)(c16 * 128 + (((7 - 4 * g - q) ^ (c16 & 7)) * 16)));
#pragma unroll
      for (int e = 0; e < 5; ++e) *lp<bf16x8>(tw, e * 2048) = tv[g][e];
    }
    CBAR();
    const __amdgpu_buffer_rsrc_t r_ws = mk_rsrc(ws, WS_END);
    const u32 tr = opq(sc + (u32)((lane >> 3) * 128 + (((lane & 7) ^ (lane >> 3)) * 16)));
    const u32 uo = WS_UT + (u32)((tch + (lane >> 3)) * 4096 + (248 - 8 * (sidx >> 1) + (lane & 7)) * 16);
#pragma unroll
    for (int k = 0; k < 10; ++k) __builtin_amdgcn_raw_buffer_store_b128(*lp<u32x4>(tr, k * 1024), r_ws, uo + (u32)(k * 8 * 4096), 0, 16);
  } else {
    __builtin_amdgcn_s_setprio(2);
  }
  STAMP(1);
  f32x16 kacc;
  f32x4 kp4[3];
  u32 pfw[7] = {};
  kgen(kacc, praw, px, bf2f(ldt), bf2f(ldtx), w * SC_SZ, L_T + w * T_SZ, lane, [&](const f32x16& kx) {
    kp4[0] = f32x4{kx[0], kx[1], kx[2], kx[3]}; kp4[1] = f32x4{kx[4], kx[5], kx[6], kx[7]}; kp4[2] = f32x4{kx[8], kx[9], kx[10], kx[11]};
  }, [&](int ps) {
    if (ps == 0 && w >= 4) { WAITV(0); gflag_st_last(faddr(F_UC), 3, s_ws, WS_FUT + (u32)(sidx * 256), tag); __builtin_amdgcn_s_setprio(1); }
    if (ps < 2) {
      const u32x4 s_p = mk_srd(kargs()->w_sp, SEQ * SEQ * 2);
      const u32 vo = (u32)((sidx >> 4) * 512 * 4096 + (((sidx & 7) * 8 + w) * 64 + lane) * 128);
      asm volatile("buffer_load_dword %0, %2, %3, %4 offen\n\tbuffer_load_dword %1, %2, %3, %5 offen"
                   : "=&v"(pfw[2 * ps]), "=&v"(pfw[2 * ps + 1]) : "v"(vo), "s"(s_p), "s"(ps * 1048576u), "s"(ps * 1048576u + 524288u) : "memory");
    }
    if (ps == 0) {
      const u32 ln = (u32)(((sidx & 7) * 8 + w) * 64 + lane);
      asm volatile("buffer_load_dword %0, %1, %2, 0 offen" : "=v"(pfw[4]) : "v"((ln / 6) * 3072 + (ln % 6) * 128 + ((sidx >> 3) & 1) * 768), "s"(mk_srd(kargs()->w_in, DM * FF * 2)) : "memory");
    }
    if (ps >= 2) {
      const u32 ln = (u32)(((sidx & 7) * 8 + w) * 36 + min(lane, 35) + (ps - 2) * 2304);
      asm volatile("buffer_load_dword %0, %1, %2, 0 offen" : "=v"(pfw[3 + ps]) : "v"((ln / 6) * 1280 + (ln % 6) * 128 + ((sidx >> 3) & 1) * 512), "s"(mk_srd(kargs()->w_out, FH * DM * 2)) : "memory");
    }
  });
#pragma unroll
  for (int k = 0; k < 3; ++k) *lp<f32x4>(w * SC_SZ + 1024 + lane * 16, k * 1024) = kp4[k];
  flag_inc(faddr(F_PC));
  expand(kacc, bf2f(dsk), w * SC_SZ, L_T + w * T_SZ, lane);
  STAMP(2);
  gflag_wait(s_ws, WS_FUT + (u32)((2 * (lane & 31) + (sidx >> 5)) * 256), tag);
  asm volatile("" ::"v"(pfw[0]), "v"(pfw[1]), "v"(pfw[2]), "v"(pfw[3]), "v"(pfw[4]), "v"(pfw[5]), "v"(pfw[6]));
  flag_wait<2>(faddr(F_UC), 4);
  STAMP(3);
  u_fetch(ws, w, c0 + w, lane);
  __builtin_amdgcn_s_setprio(0);
  STAMP(4);
  conv<0xFF>(ws, w, c0 + w, tag, sidx, lane);
  STAMP(5);
  if (w < 4) {
    if (w < 2) {
      __builtin_amdgcn_s_setprio(2);
      flag_wait<2>(faddr(F_PC), 8);
#pragma unroll
      for (int k = 0; k < 3; ++k) {
        const u32 pb = (u32)(w * 4 * SC_SZ + 1024 + k * 1024 + lane * 16);
        const f32x4 v = (*lp<f32x4>(pb) + *lp<f32x4>(pb, SC_SZ)) + (*lp<f32x4>(pb, 2 * SC_SZ) + *lp<f32x4>(pb, 3 * SC_SZ));
#pragma unroll
        for (int r = 0; r < 4; ++r) kacc[4 * k + r] = v[r];
      }
      flag_inc(faddr(F_PC));
      expand(kacc, bf2f(dskx), w * SC_SZ, L_T + (w + 8) * T_SZ, lane);
      flag_wait<2>(faddr(F_PC), 10);
      u_fetch(ws, w + 8, c0 + w + 8, lane);
      flag_inc(faddr(F_XT));
      __builtin_amdgcn_s_setprio(0);
    }
    flag_wait<4>(faddr(F_XT), 2);
    if (w & 1) conv<0xC3>(ws, 8 + (w >> 1), c0 + 8 + (w >> 1), tag, sidx, lane);
    else conv<0x3C>(ws, 8 + (w >> 1), c0 + 8 + (w >> 1), tag, sidx, lane);
  }
  WAITV(0);
  gflag_st_last(faddr(F_ACNT), 7, s_ws, WS_FA + (u32)(sidx * 256), tag);
  STAMP(6);
  TSFLUSH();
}

DEV void a_load(u32x4 (&R)[4][4], u32x4 srd, u32 voff, u32 sbase, int n) {
  bload(R[n >> 2][n & 3], srd, voff, sbase + (u32)((32 * (n >> 3) + 4 * ((n >> 2) & 1) + (n & 3)) * 4096));
}
DEV void a_take(bf16x8 (&A)[8][4], const TUnit& U, int un) {
#pragma unroll
  for (int g = 0; g < 4; ++g) A[4 * (un >> 2) + g][un & 3] = as8(tout(U, g));
}
constexpr int NPRE = 12;
#define POLL_IT 12
#define NA_LO 10
constexpr int na(int i) { return i == 9 ? 10 : (i >= NA_LO && i <= NA_LO + 2) ? 2 : 0; }
constexpr int TU0 = NA_LO < 9 ? 10 : 12;
constexpr int nd(int i) { return (i >= 10 && i <= 17 ? 1 : 0) + (i >= 16 ? 2 : 0); }
constexpr int a_first(int i) { return i <= 0 ? 0 : a_first(i - 1) + na(i - 1); }
constexpr int n_lead(int i) { return i == POLL_IT ? 1 : i == POLL_IT + 2 ? 2 : 0; }
constexpr int n_dma(int i) { return i < 6 ? 3 : i + 4 < 20 ? 2 : 0; }
constexpr int issued(int it) { return it <= 0 ? NPRE : issued(it - 1) + n_lead(it - 1) + n_dma(it - 1) + na(it - 1) + nd(it - 1); }
constexpr int w_end(int c) { return c < 4 ? 3 * (c + 1) : issued(c - 4) + n_lead(c - 4) + n_dma(c - 4); }
constexpr int a_end(int n) {
  int i = 0;
  while (a_first(i + 1) <= n) ++i;
  return issued(i) + n_lead(i) + n_dma(i) + n - a_first(i) + 1;
}
constexpr int imin(int a, int b) { return a < b ? a : b; }
static_assert(a_first(20) == 16, "schedule");
#define WINSZ 12
constexpr int WIN = WINSZ;
static_assert(WIN <= 13, "h must be older than the loads waited for at its flag");
constexpr int N_POLL = 46, N_H0 = 58;
constexpr bool wo_at(int n) { return n >= 52 && n < 63; }
constexpr int z_younger(int n) {
  int c = (n + WIN - 1 < 63 ? n + WIN - 1 : 63) - n + (n > N_POLL && n <= N_POLL + WIN ? 1 : 0) + (n > N_H0 ? n - N_H0 : 0);
  for (int k = n - WIN; k < n; ++k) c += wo_at(k);
  return c;
}
static_assert(N_H0 == 58 && WIN == 12 && N_POLL + WIN < 64, "schedule");

DEV void wo_piece(u32x4 s_wo, int lane, int tj, int rot4, int pl, int jj, int u, u32 dst) {
  const int c16 = lane & 15, q = lane >> 4;
  dma16(s_wo, (u32)(q * 1280 + tj * 256 + (c16 ^ (2 * (q + 4 * (u & 1)))) * 16), (u32)((128 * jj + 32 * ((pl + rot4) & 3) + 4 * u) * 1280), dst + u * 1024);
}
DEV void tail3(char* ws, u32 tag, int ti, int tj, int w, int lane, u32x4 (&h0)[6], u32 pv TSARG) {
  const int c16 = lane & 15, q = lane >> 4, a4 = (lane >> 2) & 3, p4 = lane & 3, rot4 = ROT4EXPR;
  const __amdgpu_buffer_rsrc_t r_ws = mk_rsrc(ws, WS_END);
  const u32x4 s_ws = mk_srd(ws, WS_END), s_wo = mk_srd(kargs()->w_out, FH * DM * 2);
  const u32 fh3 = WS_FH + (u32)(((3 * 32 + ti) * 8 + tj) * 256);
  const u32 fh = WS_FH + (u32)((ti * 8 + min(lane & 7, 5)) * 256);
  if (tj == 5 || w >= 6) {
    WAITV(0);
    gflag_st_last(faddr(F_H3F), 7, s_ws, fh3, tag);
    STAMP(9);
    if (tj < 5 && w == 6) {
      gflag_wait(s_ws, fh + 2 * 65536, tag);
      flag_st(faddr(F_P2), 1);
      gflag_wait(s_ws, WS_FA + (u32)(min(128 * tj / 10 + (lane & 15), (128 * tj + 127) / 10) * 256), tag);
      u32x4 av[16];
#pragma unroll
      for (int k = 0; k < 16; ++k)
        av[k] = __builtin_amdgcn_raw_buffer_load_b128(r_ws, WS_A + (u32)((tj * 128 + (lane >> 3)) * 4096 + ti * 128 + (lane & 7) * 16), k * 8 * 4096, 16);
      flag_wait(faddr(F_C1), 4);
      const u32 aw = opq(L_AT + (u32)((lane >> 3) * 128 + (((lane & 7) ^ (lane >> 3)) * 16)));
#pragma unroll
      for (int k = 0; k < 16; ++k) *lp<u32x4>(aw, k * 1024) = av[k];
      flag_st(faddr(F_AT), 1);
    } else if (tj < 5) {
      gflag_wait(s_ws, fh + 3 * 65536, tag);
      flag_st(faddr(F_P3), 1);
      if (blockIdx.x == 64) {
        gflag_wait(s_ws, WS_FA + (u32)(lane * 256), tag);
        gflag_st(s_ws, WS_EPOCH, tag);
      }
    }
    STAMP(10);
  } else if (w >= 4) {
    u32 lv[4];
#pragma unroll
    for (int u = 0; u < 4; ++u) lv[u] = (u32)((16 * (w & 1) + 4 * u + q) * 1280 + tj * 256 + (c16 ^ (2 * (q + 4 * (u & 1)))) * 16 - u * 1024);
#pragma unroll
    for (int k = 0; k < 13; ++k) {
      if (k == 3) flag_wait(faddr(F_H3), 8);
      if (k == 2) { WAITV(8); gflag_st_last(faddr(F_H3F), 7, s_ws, fh3, tag); STAMP(9); }
      if (k == 7) flag_wait(faddr(F_C0), 4);
      if (k == 9) { WAITV(8); flag_inc(faddr(F_L2)); }
      m0_set((k == 0 ? L_W1B : (k < 7 ? L_W2 : L_W3) + ((k - 1) % 6) * 8192) + (w & 1) * 4096);
      u32 so = (u32)((128 * (k == 0 ? 5 : (k - 1) % 6) + 32 * (((k == 0 ? 1 : k < 7 ? 2 : 3) + rot4) & 3)) * 1280);
      asm volatile("s_nop 0" : "+s"(so));
      dma16o<0>(s_wo, lv[0], so);
      dma16o<1024>(s_wo, lv[1], so);
      dma16o<2048>(s_wo, lv[2], so);
      dma16o<3072>(s_wo, lv[3], so);
    }
    WAITV(0);
    flag_inc(faddr(F_L3));
    STAMP(10);
  } else {
    const int kk = w >> 1, mt = 2 * (w & 1) + kk;
    const u32 hb = WS_H + (u32)(ti * 4 * 24576 + kk * 3072 + lane * 16);
    u32x4 hf[4][6];
    auto hload = [&](int p) {
#pragma unroll
      for (int j = 0; j < 6; ++j) hf[p][j] = __builtin_amdgcn_raw_buffer_load_b128(r_ws, hb, (u32)((mt ^ (j & 1)) * 24576 + p * 6144 + (j >> 1) * 1024), 16);
    };
#pragma unroll
    for (int j = 0; j < 6; ++j) hf[0][j] = h0[j];
    f32x4 o[2][8];
    const u32 wrd = (u32)((4 * q + a4) * 256 + (((4 * q + a4) & 7) * 32) + 8 * p4);
    u32 ad[3][8];
#pragma unroll
    for (int e = 0; e < 8; ++e) {
      ad[0][e] = opq((wrd ^ (u32)(32 * e)) + L_W0 + kk * 24576);
      ad[1][e] = opq((wrd ^ (u32)(32 * e)) + L_W2 + kk * 24576);
      ad[2][e] = opq((wrd ^ (u32)(32 * e)) + (kk ? L_W1B : L_W1 + 16384));
    }
    s16x4 wh[2][8][2];
    auto w_rd = [&](int n, int k) {
      const int p = n / 3, i = n % 3;
      const u32 off = n == 5 ? 0u : (u32)((p == 1 ? L_W1 - L_W0 : 0) + i * 8192);
      wh[n & 1][k >> 1][k & 1] = ldstr(ad[n == 5 ? 2 : p == 2 ? 1 : 0][k >> 1], off + (k & 1) * 4096);
    };
    gflag_check(pv, s_ws, fh + 65536, tag);
    hload(1);
    XST(1);
#pragma unroll
    for (int k = 0; k < 16; ++k) w_rd(0, k);
    sfor<0, 12>([&](auto IN) {
      constexpr int n = decltype(IN)::value;
      if (n == 1) { WAITV(6); gflag_st_last(faddr(F_H3F), 7, s_ws, fh3, tag); XST(2); }
      if (n == 2) { flag_inc(faddr(F_C0)); flag_wait(faddr(F_H3), 8); }
      if (n == 4) { flag_inc(faddr(F_C1)); flag_wait(faddr(F_P2), 1); hload(2); XST(3); flag_wait(faddr(F_L2), 2); XST(4); }
      if (n == 7) { flag_wait(faddr(F_P3), 1); hload(3); XST(5); }
      if (n == 8) { flag_inc(faddr(F_C2)); flag_wait(faddr(F_L3), 2); XST(6); }
      if (n == 11) flag_wait(faddr(F_C2), 4);
      SCHED();
#pragma unroll
      for (int k = 0; k < 16; ++k) {
        const int m = n == 11 ? k < 8 : k & 1, e = n == 11 ? k & 7 : k >> 1;
        if (n) mfma16_ip(o[m][e], cat8(wh[n & 1][e][0], wh[n & 1][e][1]), as8(hf[n / 3][2 * (n % 3) + m]));
        else mfma16_z(o[m][e], cat8(wh[0][e][0], wh[0][e][1]), as8(hf[0][m]));
        SCHED();
        if (n < 11) w_rd(n + 1, k);
        else if (k >= 8) *lp<f32x4>(L_W2 + w * 8192 + lane * 16, e * 1024) = o[1][e];
        SCHED();
      }
    });
    flag_st(faddr(F_EX) + 4 * w, 1);
    asm volatile("s_nop 4" : "+v"(o[0][5]), "+v"(o[0][6]), "+v"(o[0][7]));
    XST(7);
    flag_wait(faddr(F_AT), 1);
    u32x2 at[8];
    {
      const u32 ab = opq(L_AT + (u32)((4 * q + a4) * 128 + (((4 * (mt >> 1) + p4) ^ ((4 * q + a4) & 7)) * 16) + 8 * (mt & 1)));
#pragma unroll
      for (int e = 0; e < 8; ++e) at[e] = __builtin_bit_cast(u32x2, ldstr(ab, 2048 * e));
    }
    const float gate = *lp<float>(L_GATE + (u32)((mt * 16 + c16) * 4));
    flag_wait(faddr(F_EX) + 4 * (w ^ 2), 1);
    const u32 ot = L_W2 + (w ^ 2) * 8192;
#pragma unroll
    for (int e = 0; e < 8; ++e) o[0][e] += *lp<f32x4>(ot + lane * 16, e * 1024);
    CBAR();
    const u32 ob = ot + (u32)(c16 * 256 + 8 * (q & 1));
#pragma unroll
    for (int e = 0; e < 8; ++e)
      *lp<u32x2>(ob + (u32)(((2 * e + (q >> 1)) ^ c16) * 16)) =
          u32x2{pk2(__builtin_fmaf(gate, bf2f(at[e].x & 0xffffu), o[0][e][0]), __builtin_fmaf(gate, bfhi(at[e].x), o[0][e][1])),
                pk2(__builtin_fmaf(gate, bf2f(at[e].y & 0xffffu), o[0][e][2]), __builtin_fmaf(gate, bfhi(at[e].y), o[0][e][3]))};
    CBAR();
    const __amdgpu_buffer_rsrc_t r_out = mk_rsrc(kargs()->out, SEQ * DM * 2);
#pragma unroll
    for (int k = 0; k < 4; ++k) {
      const u32x4 v = *lp<u32x4>(ot + (u32)((4 * k + q) * 256 + ((c16 ^ (4 * k + q)) * 16)));
      __builtin_amdgcn_raw_buffer_store_b128(v, r_out, (u32)((ti * 64 + 32 * (mt >> 1) + 8 * k + 4 * (mt & 1) + q) * 1280 + tj * 256 + c16 * 16), 0, 0);
    }
    STAMP(11);
  }
  TSFLUSH();
}

DEV void gemm_wg(char* ws, u32 ep_raw, u32 pf_raw, int ti, int tj, int w, int lane TSARG) {
  const int c16 = lane & 15, q = lane >> 4, a4 = (lane >> 2) & 3, p4 = lane & 3, mh = w & 1, nq = w >> 1;
  const __amdgpu_buffer_rsrc_t r_ws = mk_rsrc(ws, WS_END);
  const u32x4 s_ws = mk_srd(ws, WS_END);
  const KP kp = kargs();
  const u32x4 s_x = mk_srd(kp->u, SEQ * DM * 2), s_w = mk_srd(kp->w_in, DM * FF * 2), s_wsp = mk_srd(kp->w_sp, SEQ * SEQ * 2);
  const int rot = ROTEXPR, rot4 = ROT4EXPR;
  auto line_of = [&](int l) { const int r = l + rot; return (u32)(r >= 10 ? r - 10 : r); };
  const int xr = 8 * (w & 1) + (lane >> 3);
  const u32 x_v = (u32)((ti * 64 + 32 * (w >> 2) + 8 * (xr >> 2) + 4 * ((w >> 1) & 1) + (xr & 3)) * 1280 + (((lane & 7) ^ (xr >> 1)) * 16));
  const u32 w_v0 = (u32)((8 * w + q) * 3072 + ((c16 ^ (2 * (q + 4 * (w & 1)))) * 16) + tj * 256), w_v1 = w_v0 + 4 * 3072 - 1024;
  auto x_issue = [&](int l) { dma16(s_x, x_v, line_of(l) * 128, L_X + l * 8192 + w * 1024); };
  auto w_issue = [&](int c) {
    m0_set(L_RING + (c & 3) * CHUNK + w * 2048);
    u32 so = line_of(c % 10) * (64 * 3072) + (c < 10 ? FH * 2 : 0);
    asm volatile("s_nop 0" : "+s"(so));
    dma16o<0>(s_w, w_v0, so);
    dma16o<1024>(s_w, w_v1, so);
  };
  const u32 trow = (u32)(32 * mh + 8 * (c16 >> 2) + (c16 & 3));
  u32 bsp_raw;
  {
    asm volatile("buffer_load_ushort %0, %1, %2, 0 offen" : "=v"(bsp_raw)
                 : "v"((u32)((ti * 64 + 32 * (w >> 2) + 8 * (c16 >> 2) + 4 * ((w >> 1) & 1) + (c16 & 3)) * 2)), "s"(mk_srd(kp->b_sp, SEQ * 2)) : "memory");
    dma16(mk_srd(kp->gate_w, DM * 2), lane * 16, (w & 1) * 256, L_GW + (w & 1) * 256);
  }
#pragma unroll
  for (int l = 0; l < 4; ++l) { x_issue(l); w_issue(l); }
  STAMP(1);
  const u32 rd0 = L_X + (u32)(mh * 4096 + (c16 >> 3) * 1024 + (c16 & 7) * 128 + ((q ^ (c16 >> 1)) * 16));
  const u32 xb[2][2] = {{opq(rd0), opq(rd0 ^ 64)}, {opq(rd0 + 40960), opq((rd0 ^ 64) + 40960)}};
  const u32 wrow = L_RING + (u32)((8 * q + a4) * 256 + 8 * p4), wf = (u32)(a4 + 4 * (q & 1));
  u32 wb[2] = {opq(wrow + ((2 * nq) ^ wf) * 32), opq(wrow + ((2 * nq + 1) ^ wf) * 32)};
  const u32 a_v = (u32)(8 * a4 * 4096 + (4 * q + p4) * 16);
  const int ksl = (w + ti + tj) & 7;
  const u32 a_sb = (u32)(ti * 64 * 4096 + ksl * 512 + (tj & 1) * 256);
  const u32 d_v0 = (u32)((8 * (lane >> 5) + ((lane >> 3) & 3)) * 4096 + (((lane & 7) ^ (lane >> 4)) * 16)), d_v1 = (d_v0 ^ 64) + 16 * 4096;
  bf16x8 Gf[2][2];
  const u32 gxb = opq((rd0 ^ (u32)((nq >> 1) * 64)) + (nq & 1) * 2048), gwb = opq(L_GW + (u32)(q * 16 + (nq >> 1) * 64));
  auto g_read = [&](int c) { Gf[c & 1][0] = lds128(gwb + line_of(c - 10) * 128); Gf[c & 1][1] = lds128(c < 18 ? gxb : gxb + 65536, ((c - 10) & 7) * 8192); };
  bf16x8 Xf[2][2][2];
  s16x4 Wh[2][2][2][2];
  auto f_read = [&](int c, int k) {
    const int l = c % 10;
    if (k < 4) Xf[c & 1][k >> 1][k & 1] = lds128(xb[l >= 5][k >> 1], (l % 5) * 8192 + (k & 1) * 2048);
    else { const int j = k - 4; Wh[c & 1][j >> 2][(j >> 1) & 1][j & 1] = ldstr(wb[(j >> 1) & 1], (c & 3) * CHUNK + (j >> 2) * 8192 + (j & 1) * 1024); }
  };
  auto Wf = [&](int c, int s, int e) { return cat8(Wh[c & 1][s][e][0], Wh[c & 1][s][e][1]); };
  u32x4 R[4][4];
  bf16x8 A[8][4];
  TUnit U;
  u32x4 gs, s_wo = mk_srd(kp->w_out, FH * DM * 2);
  if (tj == 5) s_wo.z = 0;
  auto wo_first = [&](int k) { wo_piece(s_wo, lane, tj, rot4, k / 6, k % 6, w, (k < 6 ? L_W0 : L_W1 - 6 * 8192) + k * 8192); };
  const u32 st_v = WS_STATS + (u32)((ti * 6 + min(w, 5)) * 1024 + lane * 16);
  f32x4 acc2[2][2] = {}, acc1[2][2] = {}, ag = {};
  asm volatile("" : "+v"(acc2[0][0]), "+v"(acc2[0][1]), "+v"(acc2[1][0]), "+v"(acc2[1][1]));
  u32 tag;
  float bsp;
  asm volatile("s_waitcnt vmcnt(%5)\n\tv_readfirstlane_b32 %0, %2\n\tv_lshlrev_b32 %1, 16, %3\n\ts_nop 1"
               : "=s"(tag), "=v"(bsp) : "v"(ep_raw), "v"(bsp_raw), "v"(pf_raw), "i"(NPRE - 3) : "memory");
  tag += 1u;
  BARRIER();
#pragma unroll
  for (int k = 0; k < 12; ++k) f_read(0, k);
  sfor<0, 20>([&](auto IT) {
    constexpr int it = decltype(IT)::value, af = a_first(it), tu = (it - TU0) >> 1;
    constexpr int c0 = (it < 19 && it != 9) ? issued(it) - w_end(it + 1) : 63;
    constexpr int c1 = (it >= TU0 && it < TU0 + 8 && !(it & 1)) ? issued(it) - a_end(4 * tu + 3) : 63;
    constexpr int c2 = it == POLL_IT + 4 ? issued(it) - issued(POLL_IT + 2) - 2 : 63;
    constexpr int cw = imin(c0, imin(c1, c2)), cw10 = issued(10) - w_end(10), cws = issued(POLL_IT + 2) - issued(POLL_IT) - 1;
    static_assert(cw >= 0 && cw < 64 && cws < 64, "wait count");
    if (cw < 63) WAITV(cw);
    __builtin_amdgcn_s_waitcnt(0xC07F);
    HWBAR();
    SCHED();
    if (it == POLL_IT + 4) { gflag_st(s_ws, WS_FZ2 + (u32)((tj * 32 + ti) * 256), tag, smask(w == 0)); STAMP(5); }
    if (it == POLL_IT) bload_sc1(gs, s_ws, st_v, 0);
    if (it == POLL_IT + 2) {
      STAMP(4);
      f32x2 t = *lp<f32x2>(L_PART + lane * 8);
#pragma unroll
      for (int j = 1; j < 6; ++j) t += *lp<f32x2>(L_PART + lane * 8, 512 * j);
      const float mean = t.x * (1.0f / FH);
      const float rsd = __builtin_amdgcn_rsqf(fmaxf(__builtin_fmaf(-mean, mean, t.y * (1.0f / FH)), 0.f) + 1e-5f);
      *lp<f32x2>(L_RS + lane * 8) = f32x2{rsd, -mean * rsd};
      CBAR();
      const u32 rb = opq(L_RS + (u32)((32 * mh + 8 * q) * 8));
      f32x4 rs[4];
#pragma unroll
      for (int k = 0; k < 4; ++k) rs[k] = *lp<f32x4>(rb, 16 * k);
      const u32 zo = WS_Z2 + (u32)(((tj * 8 + 2 * nq) * 64 + 4 * (ti >> 1) + q) * 1024 + (c16 + 16 * (2 * (ti & 1) + mh)) * 16);
#pragma unroll
      for (int e = 0; e < 2; ++e) {
        u32 o[4];
#pragma unroll
        for (int k = 0; k < 4; ++k)
          o[k] = pk2(__builtin_fmaf(acc2[k >> 1][e][2 * (k & 1)], rs[k][0], rs[k][1]), __builtin_fmaf(acc2[k >> 1][e][2 * (k & 1) + 1], rs[k][2], rs[k][3]));
        __builtin_amdgcn_raw_buffer_store_b128(u32x4{o[0], o[1], o[2], o[3]}, r_ws, zo + e * 65536, 0, 16);
      }
      SCHED();
    }
    if (it == 9) {
      const u32 blk = (u32)(2 * ((nq + rot4) & 3));
      asm volatile("" : "+v"(acc1[0][0]), "+v"(acc1[0][1]), "+v"(acc1[1][0]), "+v"(acc1[1][1]), "+v"(ag));
      wb[0] = opq(wrow + (blk ^ wf) * 32);
      wb[1] = opq(wrow + ((blk + 1) ^ wf) * 32);
    }
    const u32 wso = line_of((it + 4) % 10) * (64 * 3072) + (it + 4 < 10 ? FH * 2 : 0);
    auto vmem = [&](int j) {
      const int jw = j - (it < 6 ? 1 : 0), ja = j - n_dma(it);
      if (it < 6 && j == 0) x_issue(it + 4);
      else if (j < n_dma(it) && jw == 0) { m0_set(L_RING + (it & 3) * CHUNK + w * 2048); asm volatile("s_nop 0"); dma16o<0>(s_w, w_v0, wso); }
      else if (j < n_dma(it)) dma16o<1024>(s_w, w_v1, wso);
      else if (ja < na(it)) a_load(R, s_wsp, a_v, a_sb, (af + ja) & 15);
      else if (ja - na(it) < nd(it)) {
        const int ja = j - n_dma(it) - na(it);
        const bool inx = it <= 17 && ja == 0;
        const int pc = inx ? it - 10 : 8 + 2 * (it - 16) + ja - (it <= 17 ? 1 : 0);
        dma16(s_wsp, (pc & 1) ? d_v1 : d_v0, (a_sb ^ 256u) + (u32)((32 * (pc >> 3) + 4 * ((pc >> 2) & 1)) * 4096 + ((pc >> 1) & 1) * 128),
              inx ? L_X + pc * 8192 + w * 1024 : L_RING + (it & 3) * CHUNK + w * 2048 + (pc & 1) * 1024);
      }
    };
    SCHED();
#pragma unroll
    for (int k = 0; k < 8; ++k) {
      const int s = k >> 2, b = (k >> 1) & 1, e = k & 1;
      if (it < 10) mfma16_ip(acc2[b][e], Xf[it & 1][s][b], Wf(it, s, e));
      else mfma16_ip(acc1[b][e], Wf(it, s, e), Xf[it & 1][s][b]);
      SCHED();
      if (it < 19 && it != 9 && k < 6) { f_read(it + 1, 2 * k); f_read(it + 1, 2 * k + 1); }
      if (!(k & 1)) vmem(k >> 1);
      if (k == 7 && it != 9) vmem(4);
      if (it >= 10 && it < 19 && k == 5) g_read(it + 1);
      if (it >= TU0 && it < TU0 + 8 && k >= 6) {
        tstep(U, R[tu & 3], 2 * (it & 1) + k - 6);
        if ((it & 1) && k == 7) a_take(A, U, tu & 3);
      }
      SCHED();
    }
    if (it >= 10) mfma16_ip(ag, Gf[it & 1][0], Gf[it & 1][1]);
    if (it == 9) {
      STAMP(2);
      float sv[16];
      asm volatile("s_nop 7" : "+v"(acc2[0][0]), "+v"(acc2[0][1]), "+v"(acc2[1][0]), "+v"(acc2[1][1]));
#pragma unroll
      for (int k = 0; k < 8; ++k) {
        const float g0 = gelu_f(acc2[k >> 2][0][k & 3]), g1 = gelu_f(acc2[k >> 2][1][k & 3]);
        acc2[k >> 2][0][k & 3] = g0;
        acc2[k >> 2][1][k & 3] = g1;
        sv[2 * k] = g0 + g1;
        sv[2 * k + 1] = __builtin_fmaf(g0, g0, g1 * g1);
        if (k & 1) { a_load(R, s_wsp, a_v, a_sb, (af + 2 + (k >> 1)) & 15); SCHED(); }
      }
      *lp<f32x4>(L_PART + (u32)(nq * 512 + (32 * mh + 8 * q) * 8 + a4 * 16)) = rowsum16(sv);
      SCHED();
      WAITV(cw10 - 4);
      BARRIER();
      if (w == 0) {
        const u32 pb = L_PART + lane * 8;
        const f32x2 t = (*lp<f32x2>(pb) + *lp<f32x2>(pb, 512)) + (*lp<f32x2>(pb, 1024) + *lp<f32x2>(pb, 1536));
        __builtin_amdgcn_raw_buffer_store_b128(u32x4{__float_as_uint(t.x), tag, __float_as_uint(t.y), tag}, r_ws, WS_STATS + (u32)((ti * 6 + tj) * 1024 + lane * 16), 0, 16);
      }
#pragma unroll
      for (int k = 0; k < 12; ++k) f_read(10, k);
      g_read(10);
#pragma unroll
      for (int k = 6; k < 10; ++k) a_load(R, s_wsp, a_v, a_sb, (af + k) & 15);
      STAMP(3);
    }
    if (it == POLL_IT + 1) {
      WAITV_PIN(cws, gs);
      u32x4 g = gs;
#pragma nounroll
      for (u32 sp = 0; sp < 0x4000u && !__all(g.y == tag && g.w == tag); ++sp) {
        __builtin_amdgcn_s_sleep(2);
        g = __builtin_amdgcn_raw_buffer_load_b128(r_ws, st_v, 0, 16);
      }
      *lp<u32x2>(L_PART + (u32)(min(w, 5) * 512 + lane * 8)) = u32x2{g.x, g.z};
    }
  });
  asm volatile("s_nop 7" : "+v"(acc1[0][0]), "+v"(acc1[0][1]), "+v"(acc1[1][0]), "+v"(acc1[1][1]), "+v"(ag));
  STAMP(6);
  gflag_wait(s_ws, WS_FZ2 + (u32)((tj * 32 + 4 * ksl + (lane & 3)) * 256), tag);
  STAMP(7);
  {
    const u32 db = (u32)((q >> 1) * 16384 + (c16 & 7) * 128 + ((4 * (q & 1)) ^ (c16 >> 1)) * 16);
#pragma unroll
    for (int e = 0; e < 4; ++e) {
      const u32 ax = opq((db ^ (u32)(16 * e)) + L_X + w * 1024 + (c16 >> 3) * 8192), ar = opq((db ^ (u32)(16 * e)) + L_RING + w * 2048 + (c16 >> 3) * 1024);
      A[4 + e][0] = lds128(ax); A[4 + e][1] = lds128(ax, 32768); A[4 + e][2] = lds128(ar); A[4 + e][3] = lds128(ar, 32768);
    }
  }
  const u32 zb = WS_Z2 + (u32)((tj * 8 * 64 + 8 * ksl) * 1024 + lane * 16);
  u32x4 win[WIN];
  auto zload = [&](int n) { bload_sc1(win[n % WIN], s_ws, zb, (u32)((((n >> 4) + rot4) & 3) * 131072 + ((n >> 3) & 1) * 65536 + ((n & 7) ^ (4 * (tj & 1))) * 1024)); };
  *lp<float>(L_GP + (nq >> 1) * 256 + trow * 4 + (nq & 1) * 16) = ag[0];
#pragma unroll
  for (int k = 0; k < 4; ++k) {
    const int b = k >> 1, e = k & 1;
    zload(3 * k); zload(3 * k + 1); zload(3 * k + 2);
    *lp<u32x2>(L_Z1 + (u32)((2 * mh * 4 + nq) * 1024 + lane * 8), b * 4096 + e * 512) =
        u32x2{pk2(gelu_f(acc1[b][e][0]), gelu_f(acc1[b][e][1])), pk2(gelu_f(acc1[b][e][2]), gelu_f(acc1[b][e][3]))};
    SCHED();
  }
  const u32 rwb = opq(L_RED + (u32)(w * 4096 + lane * 16));
  const u32 rrb = opq(L_RED + (u32)((lane >> 5) * 16384 + (w >> 1) * 1024 + (32 * (w & 1) + (lane & 31)) * 16));
  f32x4 rv[4], se;
  u32x2 z1p[4];
  float og;
  auto z1_fetch = [&]() {
#pragma unroll
    for (int p = 0; p < 4; ++p) z1p[p] = *lp<u32x2>(L_Z1 + (u32)((w >> 1) * 4096 + (lane >> 5) * 512 + (32 * (w & 1) + (lane & 31)) * 8), p * 1024);
    const u32 gb = L_GP + (u32)((32 * (w >> 2) + 8 * (c16 >> 2) + 4 * ((w >> 1) & 1) + (c16 & 3)) * 4);
    rv[0][0] = *lp<float>(gb);
    rv[0][1] = *lp<float>(gb, 256);
  };
  auto gate_fin = [&]() {
    const float gate = __builtin_amdgcn_rcpf(1.0f + __builtin_amdgcn_exp2f((rv[0][0] + rv[0][1]) * -1.4426950409f));
    *lp<float>(L_GATE + (u32)(((w >> 1) * 16 + c16) * 4)) = gate;
    og = 1.0f - gate;
    bsp *= og;
    asm volatile("" : "+v"(og), "+v"(bsp));
  };
  const u32 hoff = lane < 32 ? WS_H + (u32)(((ti * 4 + (w >> 1)) * 4 * 6 + tj) * 1024 + (32 * (w & 1) + lane) * 16) : WS_END;
  auto red_ld = [&](int e) {
#pragma unroll
    for (int k = 0; k < 4; ++k) rv[k] = *lp<f32x4>(rrb, (e & 1) * 32768 + k * 4096);
  };
  auto red_add = [&](int e) {
    const f32x4 t = (rv[0] + rv[1]) + (rv[2] + rv[3]);
    if (!(e & 1)) { se = t; asm volatile("" : "+v"(se)); return; }
    float g[4];
#pragma unroll
    for (int r = 0; r < 4; ++r) {
      const auto x = __builtin_amdgcn_permlane32_swap(__float_as_uint(se[r]), __float_as_uint(t[r]), false, false);
      g[r] = __builtin_fmaf(__uint_as_float(x[0]) + __uint_as_float(x[1]), og, bsp);
    }
    const u32x2 z = z1p[e >> 1];
    const u32 o0 = pk2(bf2f(z.x & 0xffffu) * g[0], bfhi(z.x) * g[1]), o1 = pk2(bf2f(z.y & 0xffffu) * g[2], bfhi(z.y) * g[3]);
    const auto y0 = __builtin_amdgcn_permlane32_swap(o0, o0, false, false), y1 = __builtin_amdgcn_permlane32_swap(o1, o1, false, false);
    __builtin_amdgcn_raw_buffer_store_b128(u32x4{y0[0], y1[0], y0[1], y1[1]}, r_ws, hoff + (u32)((e >> 1) * 6144), 0, 16);
  };
  const u32 w0mask = smask(w == 0);
  f32x4 acc[2][4];
  u32x4 h0[6], s_h = s_ws;
  if (w >= 4 || tj == 5) s_h.z = 0;
  u32 pv;
  const u32 fh0 = WS_FH + (u32)((ti * 8 + min(lane & 7, 5)) * 256);
  sfor<0, 64>([&](auto IN) {
    constexpr int n = decltype(IN)::value, e = n >> 3, ks = n & 7;
    constexpr int zy = z_younger(n);
    WAITV_PIN(zy, win[n % WIN]);
    const bf16x8 zf = as8(win[n % WIN]);
#pragma unroll
    for (int m = 0; m < 4; ++m) { if (ks) mfma16_ip(acc[e & 1][m], zf, A[ks][m]); else mfma16_z(acc[e & 1][m], zf, A[ks][m]); }
    SCHED();
    if (n + WIN < 64) zload(n + WIN);
    if (e > 0 && ks < 4) *lp<f32x4>(rwb, ((e - 1) & 1) * 32768 + ks * 1024) = acc[(e - 1) & 1][ks & 3];
    if (ks == 5) {
      WAITL0();
      HWBAR();
      if (e == 4 || e == 6) gflag_st(s_ws, WS_FH + (u32)((((e - 4) / 2 * 32 + ti) * 8 + tj) * 256), tag, w0mask);
      if (e > 0) red_ld(e - 1); else z1_fetch();
    }
    if (wo_at(n)) wo_first((n + 3) % 11);
    if (n == N_POLL) asm volatile("buffer_load_dword %0, %1, %2, 0 offen sc1" : "=v"(pv) : "v"(fh0), "s"(s_ws) : "memory");
    if (n == N_H0) { WAITV(63 - N_POLL - WIN + 1 + 7); gflag_check(pv, s_ws, fh0, tag); }
    if (n >= N_H0) bload_sc1(h0[(n + 60 - N_H0) % 6], s_h, (rwb & 1023u), WS_H + (u32)((ti * 4 + ((2 * (w & 1) + ((w >> 1) & 1)) ^ ((n - N_H0) & 1))) * 24576 + ((w >> 1) & 1) * 3072 + ((n - N_H0) >> 1) * 1024));
    if (ks == 7) { if (e > 0) red_add(e - 1); else gate_fin(); }
    SCHED();
  });
  asm volatile("s_nop 3" : "+v"(acc[1][0]), "+v"(acc[1][1]), "+v"(acc[1][2]), "+v"(acc[1][3]));
#pragma unroll
  for (int m = 0; m < 4; ++m) *lp<f32x4>(rwb, 32768 + m * 1024) = acc[1][m];
  asm volatile("buffer_load_dword %0, %1, %2, %3 offen sc1" : "=v"(pv) : "v"(fh0), "s"(s_ws), "s"(65536u) : "memory");
  WAITV(12);
  BARRIER();
  STAMP(8);
  red_ld(7);
  gflag_st(s_ws, WS_FH + (u32)(((2 * 32 + ti) * 8 + tj) * 256), tag, w0mask);
  red_add(7);
  asm volatile("s_waitcnt vmcnt(1)" : "+v"(h0[0]), "+v"(h0[1]), "+v"(h0[2]), "+v"(h0[3]), "+v"(h0[4]), "+v"(h0[5]), "+v"(pv)::"memory");
  flag_inc(faddr(F_H3));
  tail3(ws, tag, ti, tj, w, (int)opq(lane), h0, pv TSPASS);
}

__global__ __launch_bounds__(512, 2) void fused_kernel(Params P_unused) {
  extern __shared__ __attribute__((aligned(16))) char smem[];
  const int lane = threadIdx.x & 63;
  const int w = __builtin_amdgcn_readfirstlane(threadIdx.x >> 6);
  const int b = blockIdx.x, xcd = b & 7, idx = b >> 3;
  u32 pf_raw, ep_raw;
  {
    u64 pc;
    asm volatile("s_getpc_b64 %0" : "=s"(pc));
    const u32 ln = (u32)(((idx & 7) * 8 + w) * 8 + (lane & 7));
    asm volatile("global_load_dword %0, %1, %2" : "=v"(pf_raw) : "v"((ln < CODE_LINES ? ln : 0u) * 128u), "s"(pc) : "memory");
  }
  char* ws = kargs()->ws;
  STAMP(0);
  if (w == 4) *lp<u32>(L_FLG + 4 * lane) = 0;
  {
    const KP kp = kargs();
    const char* p = ws;
    const u32 st = TLBW == 1 ? (1u << 20) : (1u << 16), k = TLBW == 1 ? (u32)lane : (u32)(idx * 7 + lane);
    const bool on = lane > 0 && lane < (TLBW == 1 ? 13 : 8);
    if (on && w == 0 && k * st < WS_END) p = ws + k * st;
    if (on && w == 1 && k * st < SEQ * SEQ * 2) p = (const char*)kp->w_sp + k * st;
    if (on && w == 2 && (k - 1) * st < SEQ * DM * 2) p = (const char*)kp->out + (k - 1) * st;
    if (on && w == 3 && (k - 1) * st < FH * DM * 2) p = (const char*)kp->w_out + (k - 1) * st;
    asm volatile("global_load_dword %0, %1, off sc1" : "=v"(ep_raw) : "v"(p) : "memory");
  }
  if (idx < 8) ssm_wg(ws, ep_raw, pf_raw, xcd * 8 + idx, w, lane TSPASS);
  else gemm_wg(ws, ep_raw, pf_raw, (xcd >> 1) * 8 + (idx - 8) / 3, (xcd & 1) * 3 + (idx - 8) % 3, w, lane TSPASS);
}

extern "C" void kernel_launch(void* const* d_in, const int* in_sizes, int n_in, void* d_out, int out_size, void* d_ws, size_t ws_size,
                              hipStream_t stream) {
  Params P;
  P.u = (const u16*)d_in[0]; P.Are = (const u16*)d_in[1]; P.Aim = (const u16*)d_in[2]; P.Bre = (const u16*)d_in[3];
  P.Bim = (const u16*)d_in[4]; P.Cre = (const u16*)d_in[5]; P.Cim = (const u16*)d_in[6]; P.logdt = (const u16*)d_in[7];
  P.dskip = (const u16*)d_in[8]; P.w_in = (const u16*)d_in[10]; P.w_sp = (const u16*)d_in[11]; P.b_sp = (const u16*)d_in[12];
  P.w_out = (const u16*)d_in[13]; P.gate_w = (const u16*)d_in[14]; P.out = (u16*)d_out; P.ws = (char*)d_ws;
  (void)hipFuncSetAttribute((const void*)fused_kernel, hipFuncAttributeMaxDynamicSharedMemorySize, L_TOTAL);
  fused_kernel<<<256, 512, L_TOTAL, stream>>>(P);
}
```

```cpp
#include <hip/hip_runtime.h>
#include <hip/hip_bf16.h>

typedef unsigned short u16;
typedef unsigned u32;
typedef unsigned long long u64;
typedef short bf16x8 __attribute__((ext_vector_type(8)));
typedef short s16x4 __attribute__((ext_vector_type(4)));
typedef float f32x2 __attribute__((ext_vector_type(2)));
typedef float f32x4 __attribute__((ext_vector_type(4)));
typedef float f32x16 __attribute__((ext_vector_type(16)));
typedef unsigned u32x2 __attribute__((ext_vector_type(2)));
typedef unsigned u32x4 __attribute__((ext_vector_type(4)));
typedef __bf16 bf2_t __attribute__((ext_vector_type(2)));
typedef _Float16 h2_t __attribute__((ext_vector_type(2)));
typedef _Float16 h8_t __attribute__((ext_vector_type(8)));
#define LAS __attribute__((address_space(3)))
#define DEV __device__ __forceinline__
#define SCHED() __builtin_amdgcn_sched_barrier(0)
#define CBAR() asm volatile("" ::: "memory")
#define WAITV(n) asm volatile("s_waitcnt vmcnt(%0)" ::"i"(n) : "memory")
#define WAITL0() asm volatile("s_waitcnt lgkmcnt(0)" ::: "memory")
#define HWBAR() __builtin_amdgcn_s_barrier()

constexpr int SEQ = 2048, DM = 640, NST = 192, FF = 1536, FH = 768;

constexpr u32 WS_EPOCH = 0;
constexpr u32 WS_FZ2 = 4096;
constexpr u32 WS_FA = WS_FZ2 + 256 * 256;
constexpr u32 WS_FUT = WS_FA + 64 * 256;
constexpr u32 WS_FH = WS_FUT + 64 * 256;
constexpr u32 WS_STATS = WS_FH + 4 * 256 * 256;
constexpr u32 WS_Z2 = 1u << 20;
constexpr u32 WS_H = WS_Z2 + SEQ * FH * 2;
constexpr u32 WS_A = WS_H + SEQ * FH * 2;
constexpr u32 WS_UT = WS_A + SEQ * DM * 2;
constexpr u32 WS_END = WS_UT + SEQ * DM * 2;
static_assert(WS_STATS + 32 * 6 * 1024 <= WS_Z2, "ws map");

constexpr u32 L_TOTAL = 163840, L_FLG = L_TOTAL - 256, L_GATE = L_FLG - 256;
constexpr u32 L_X = 0;
constexpr u32 L_RING = 81920, CHUNK = 16384;
constexpr u32 L_GW = 155648;
constexpr u32 L_PART = L_GW + 1280;
constexpr u32 L_RS = L_PART + 3072;
constexpr u32 L_GP = L_RS + 512;
static_assert(L_GP + 1024 <= L_GATE, "lds");
constexpr u32 L_Z1 = 65536;
constexpr u32 L_RED = 0;
constexpr u32 L_W0 = 65536, L_W1 = 114688, L_W1B = 0, L_W2 = 16384, L_W3 = L_W0;
constexpr u32 L_AT = L_W1;
constexpr u32 SC_SZ = 8704, L_T = 8 * SC_SZ, T_SZ = 4608, U_SZ = 4224, L_UA = L_T + 10 * T_SZ;
static_assert(L_UA % 128 == 0 && L_UA + 10 * U_SZ <= L_GATE && SC_SZ % 128 == 0, "lds");
DEV u32 ub_base(int k) { return k < 8 ? k * SC_SZ + 4288 : (k - 8) * SC_SZ + 64; }
enum { F_H3 = 0, F_H3F = 1, F_C0 = 2, F_C1 = 3, F_L2 = 4, F_L3 = 5, F_AT = 6, F_AR = 7, F_XT = 8, F_ACNT = 9, F_P1 = 10, F_P2 = 11, F_P3 = 12, F_C2 = 13, F_UC = 14, F_PC = 15, F_EX = 16 };
#define SPRIO 2
#define ROT4EXPR 0
#define ROTEXPR (ti + 3 * tj) % 10

#define STAMPS(set, k)
#define TSARG
#define TSPASS
#define TSFLUSH()

#define STAMP(k) STAMPS(0, k)

struct Params {
  const u16 *u, *Are, *Aim, *Bre, *Bim, *Cre, *Cim, *logdt, *dskip, *w_in, *w_sp, *b_sp, *w_out, *gate_w;
  u16* out;
  char* ws;
};

typedef const __attribute__((address_space(4))) Params* KP;
DEV KP kargs() {
  KP p = (KP)__builtin_amdgcn_kernarg_segment_ptr();
  asm volatile("" : "+s"(p));
  return p;
}
DEV float bf2f(u32 v) { return __uint_as_float(v << 16); }
DEV float bfhi(u32 v) { return __uint_as_float(v & 0xffff0000u); }
DEV u32 pk2(float a, float b) { return __builtin_bit_cast(u32, __builtin_convertvector(f32x2{a, b}, bf2_t)); }
DEV u32x4 mk_srd(const void* p, u32 bytes) { return u32x4{(u32)(size_t)p, (u32)((size_t)p >> 32) & 0xffffu, bytes, 0x00020000u}; }
DEV __amdgpu_buffer_rsrc_t mk_rsrc(const void* p, u32 bytes) { return __builtin_amdgcn_make_buffer_rsrc((void*)p, 0, bytes, 0x00020000); }
DEV void dma16(u32x4 srd, u32 voff, u32 soff, u32 dst) {
  asm volatile("s_mov_b32 m0, %2\n\ts_nop 0\n\tbuffer_load_dwordx4 %0, %1, %3 offen lds" ::"v"(voff), "s"(srd), "s"(dst), "s"(soff) : "memory");
}
DEV void m0_set(u32 dst) { asm volatile("s_mov_b32 m0, %0" ::"s"(dst) : "memory"); }
template <int OFF> DEV void dma16o(u32x4 srd, u32 voff, u32 soff) {
  asm volatile("buffer_load_dwordx4 %0, %1, %2 offen offset:%3 lds" ::"v"(voff), "s"(srd), "s"(soff), "i"(OFF) : "memory");
}
DEV void bload(u32x4& d, u32x4 srd, u32 voff, u32 soff) {
  asm volatile("buffer_load_dwordx4 %0, %1, %2, %3 offen" : "=v"(d) : "v"(voff), "s"(srd), "s"(soff) : "memory");
}
DEV void bload_sc1(u32x4& d, u32x4 srd, u32 voff, u32 soff) {
  asm volatile("buffer_load_dwordx4 %0, %1, %2, %3 offen sc1" : "=v"(d) : "v"(voff), "s"(srd), "s"(soff) : "memory");
}
#define WAITV_PIN(n, x) asm volatile("s_waitcnt vmcnt(%1)" : "+v"(x) : "i"(n) : "memory")
template <class T> DEV LAS T* lp(u32 base, u32 off = 0) { return (LAS T*)((LAS char*)(size_t)base + off); }
DEV u32 smask(bool c) { u32 m = __builtin_amdgcn_readfirstlane(c ? 1 : 0); asm volatile("s_nop 0" : "+s"(m)); return m; }
DEV u32 opq(u32 x) { asm volatile("" : "+v"(x)); return x; }
DEV bf16x8 lds128(u32 base, u32 off = 0) { return *lp<bf16x8>(base, off); }
DEV s16x4 ldstr(u32 base, u32 off = 0) { return __builtin_amdgcn_ds_read_tr16_b64_v4i16(lp<s16x4>(base, off)); }
DEV bf16x8 cat8(s16x4 a, s16x4 b) { return __builtin_shufflevector(a, b, 0, 1, 2, 3, 4, 5, 6, 7); }
DEV bf16x8 as8(u32x4 v) { return __builtin_bit_cast(bf16x8, v); }

DEV u32 faddr(int k) { return L_FLG + 4 * k; }
DEV void flag_st(u32 addr, u32 v) { asm volatile("ds_write_b32 %0, %1" ::"v"(addr), "v"(v) : "memory"); }
DEV void flag_inc(u32 addr) {
  asm volatile("s_mov_b64 exec, 1\n\tds_add_u32 %0, %1\n\ts_mov_b64 exec, -1" ::"v"(addr), "v"(1u) : "memory");
}
DEV void flag_pre(u32& pre, u32 addr) { asm volatile("ds_read_b32 %0, %1" : "=v"(pre) : "v"(addr) : "memory"); }
template <int SLEEP = 1> DEV void flag_wait_pre(u32 pre, u32 addr, u32 want) {
  u32 t, cnt, v;
  asm volatile("s_waitcnt lgkmcnt(0)\n\t"
               "v_readfirstlane_b32 %0, %6\n\t"
               "s_cmp_ge_u32 %0, %4\n\t"
               "s_cbranch_scc1 .Lfd%=\n\t"
               "s_mov_b32 %1, 0x20000\n"
               ".Lfw%=:\n\t"
               "s_sleep %5\n\t"
               "ds_read_b32 %2, %3\n\t"
               "s_sub_u32 %1, %1, 1\n\t"
               "s_cbranch_scc1 .Lfe%=\n\t"
               "s_waitcnt lgkmcnt(0)\n\t"
               "v_readfirstlane_b32 %0, %2\n\t"
               "s_cmp_lt_u32 %0, %4\n\t"
               "s_cbranch_scc1 .Lfw%=\n"
               ".Lfe%=:\n\t"
               "s_waitcnt lgkmcnt(0)\n"
               ".Lfd%=:"
               : "=&s"(t), "=&s"(cnt), "=&v"(v) : "v"(addr), "s"(want), "i"(SLEEP), "v"(pre) : "memory", "scc");
}
template <int SLEEP = 1> DEV void flag_wait(u32 addr, u32 want) {
  u32 pre;
  flag_pre(pre, addr);
  flag_wait_pre<SLEEP>(pre, addr, want);
}
DEV void gflag_st(u32x4 srd, u32 off, u32 tag, u32 mask = 1) {
  asm volatile("s_mov_b32 exec_lo, %3\n\ts_mov_b32 exec_hi, 0\n\tbuffer_store_dword %0, off, %1, %2 sc1\n\ts_mov_b64 exec, -1" ::"v"(tag), "s"(srd), "s"(off), "s"(mask) : "memory");
}
DEV void gflag_st_last(u32 addr, u32 last, u32x4 srd, u32 off, u32 tag) {
  u32 old, t;
  u64 m;
  asm volatile("s_mov_b64 exec, 1\n\t"
               "ds_add_rtn_u32 %0, %3, %4\n\t"
               "s_waitcnt lgkmcnt(0)\n\t"
               "v_readfirstlane_b32 %1, %0\n\t"
               "s_cmp_eq_u32 %1, %5\n\t"
               "s_cselect_b64 %2, 1, 0\n\t"
               "s_mov_b64 exec, %2\n\t"
               "buffer_store_dword %8, off, %6, %7 sc1\n\t"
               "s_mov_b64 exec, -1"
               : "=&v"(old), "=&s"(t), "=&s"(m) : "v"(addr), "v"(1u), "s"(last), "s"(srd), "s"(off), "v"(tag) : "memory", "scc");
}
DEV void gflag_wait(u32x4 srd, u32 voff, u32 tag) {
  u32 v, cnt;
  asm volatile("s_mov_b32 %1, 0x4000\n"
               ".Lgw%=:\n\t"
               "buffer_load_dword %0, %2, %3, 0 offen sc1\n\t"
               "s_waitcnt vmcnt(0)\n\t"
               "v_cmp_ne_u32 vcc, %4, %0\n\t"
               "s_cbranch_vccz .Lgd%=\n\t"
               "s_sleep 2\n\t"
               "s_sub_u32 %1, %1, 1\n\t"
               "s_cbranch_scc0 .Lgw%=\n"
               ".Lgd%=:"
               : "=&v"(v), "=&s"(cnt) : "v"(voff), "s"(srd), "s"(tag) : "memory", "scc", "vcc");
}

DEV void gflag_check(u32 v0, u32x4 srd, u32 voff, u32 tag) {
  u32 v, cnt;
  asm volatile("v_cmp_ne_u32 vcc, %4, %5\n\t"
               "s_cbranch_vccz .Lgd%=\n\t"
               "s_mov_b32 %1, 0x4000\n"
               ".Lgw%=:\n\t"
               "buffer_load_dword %0, %2, %3, 0 offen sc1 ; half 1\n\t"
               "s_waitcnt vmcnt(0) ; half 1\n\t"
               "v_cmp_ne_u32 vcc, %4, %0\n\t"
               "s_cbranch_vccz .Lgd%=\n\t"
               "s_sleep 2\n\t"
               "s_sub_u32 %1, %1, 1\n\t"
               "s_cbranch_scc0 .Lgw%=\n"
               ".Lgd%=:"
               : "=&v"(v), "=&s"(cnt) : "v"(voff), "s"(srd), "s"(tag), "v"(v0) : "memory", "scc", "vcc");
}

DEV f32x4 gelu4(f32x4 a) {
  const float k0 = -2.3022081999f, k1 = -0.1029432397f;
  const f32x4 y = a * __builtin_elementwise_fma(a * a, f32x4{k1, k1, k1, k1}, f32x4{k0, k0, k0, k0});
  const f32x4 d = f32x4{__builtin_amdgcn_exp2f(y[0]), __builtin_amdgcn_exp2f(y[1]), __builtin_amdgcn_exp2f(y[2]), __builtin_amdgcn_exp2f(y[3])} + 1.0f;
  return a * f32x4{__builtin_amdgcn_rcpf(d[0]), __builtin_amdgcn_rcpf(d[1]), __builtin_amdgcn_rcpf(d[2]), __builtin_amdgcn_rcpf(d[3])};
}
template <int CTRL> DEV float dppf(float x) { return __builtin_bit_cast(float, __builtin_amdgcn_update_dpp(0, __builtin_bit_cast(int, x), CTRL, 0xf, 0xf, false)); }
#define MFMA16(a, b, c) __builtin_amdgcn_mfma_f32_16x16x32_bf16(a, b, c, 0, 0, 0)
#define MFMA32H(a, b, c) __builtin_amdgcn_mfma_f32_32x32x16_f16(a, b, c, 0, 0, 0)

DEV f32x4 rowsum16(const float (&v)[16]) {
  float a[8], o0, o1, o2, o3;
#define RS1(d, x, y) "v_add_f32_dpp " d ", " x ", " x " row_ror:8 row_mask:0xf bank_mask:0x3\n\tv_add_f32_dpp " d ", " y ", " y " row_ror:8 row_mask:0xf bank_mask:0xc\n\t"
  asm volatile("s_nop 1\n\t" RS1("%0", "%8", "%16") RS1("%1", "%9", "%17") RS1("%2", "%10", "%18") RS1("%3", "%11", "%19")
               RS1("%4", "%12", "%20") RS1("%5", "%13", "%21") RS1("%6", "%14", "%22") RS1("%7", "%15", "%23")
               : "=&v"(a[0]), "=&v"(a[1]), "=&v"(a[2]), "=&v"(a[3]), "=&v"(a[4]), "=&v"(a[5]), "=&v"(a[6]), "=&v"(a[7])
               : "v"(v[0]), "v"(v[1]), "v"(v[2]), "v"(v[3]), "v"(v[4]), "v"(v[5]), "v"(v[6]), "v"(v[7]),
                 "v"(v[8]), "v"(v[9]), "v"(v[10]), "v"(v[11]), "v"(v[12]), "v"(v[13]), "v"(v[14]), "v"(v[15]));
#define RS2(d, x, y) "v_add_f32_dpp " d ", " x ", " x " row_half_mirror row_mask:0xf bank_mask:0x5\n\tv_add_f32_dpp " d ", " y ", " y " row_half_mirror row_mask:0xf bank_mask:0xa\n\t"
#define RS3(qp) "v_add_f32_dpp %0, %0, %0 quad_perm:" qp " row_mask:0xf bank_mask:0xf\n\tv_add_f32_dpp %1, %1, %1 quad_perm:" qp " row_mask:0xf bank_mask:0xf\n\t" \
                "v_add_f32_dpp %2, %2, %2 quad_perm:" qp " row_mask:0xf bank_mask:0xf\n\tv_add_f32_dpp %3, %3, %3 quad_perm:" qp " row_mask:0xf bank_mask:0xf\n\t"
  asm volatile("s_nop 1\n\t" RS2("%0", "%4", "%8") RS2("%1", "%5", "%9") RS2("%2", "%6", "%10") RS2("%3", "%7", "%11") RS3("[1,0,3,2]") RS3("[2,3,0,1]")
               : "=&v"(o0), "=&v"(o1), "=&v"(o2), "=&v"(o3)
               : "v"(a[0]), "v"(a[1]), "v"(a[2]), "v"(a[3]), "v"(a[4]), "v"(a[5]), "v"(a[6]), "v"(a[7]));
  return f32x4{o0, o1, o2, o3};
}
DEV void mfma16_ip(f32x4& c, bf16x8 a, bf16x8 b) { asm volatile("v_mfma_f32_16x16x32_bf16 %0, %1, %2, %0" : "+v"(c) : "v"(a), "v"(b)); }
DEV void mfma16_z(f32x4& c, bf16x8 a, bf16x8 b) { asm volatile("v_mfma_f32_16x16x32_bf16 %0, %1, %2, 0" : "=&v"(c) : "v"(a), "v"(b)); }
DEV h2_t hmul(h2_t x, h2_t e) { return __builtin_elementwise_fma(h2_t{x.y, x.y}, h2_t{-e.y, e.x}, h2_t{x.x, x.x} * e); }
DEV h2_t toh(f32x2 v) { return __builtin_convertvector(v, h2_t); }
DEV f32x2 csq(f32x2 e) {
  float t, x, y;
  asm("v_mul_f32 %0, %3, %3\n\tv_add_f32 %1, %2, %2\n\tv_fma_f32 %0, %2, %2, -%0\n\tv_mul_f32 %1, %1, %3" : "=&v"(x), "=&v"(y) : "v"(e.x), "v"(e.y));
  (void)t;
  return f32x2{x, y};
}
DEV f32x2 cexp2(float ar, float ai, float l) {
  const float m = __builtin_amdgcn_exp2f(ar * l), th = __builtin_amdgcn_fractf(ai * l);
  return f32x2{__uint_as_float(opq(__float_as_uint(m * __builtin_amdgcn_cosf(th)))), m * __builtin_amdgcn_sinf(th)};
}

struct TUnit { u32 t[4][4], o[4][4]; };
#define TSTEP_ASM(qp) \
  "s_mov_b32 vcc_lo, %24\n\ts_mov_b32 vcc_hi, %24\n\t" \
  "v_cndmask_b32_dpp %0, %16, %8, vcc quad_perm:" qp " row_mask:0xf bank_mask:0xf\n\t" \
  "v_cndmask_b32_dpp %1, %17, %9, vcc quad_perm:" qp " row_mask:0xf bank_mask:0xf\n\t" \
  "v_cndmask_b32_dpp %2, %18, %10, vcc quad_perm:" qp " row_mask:0xf bank_mask:0xf\n\t" \
  "v_cndmask_b32_dpp %3, %19, %11, vcc quad_perm:" qp " row_mask:0xf bank_mask:0xf\n\t" \
  "v_cndmask_b32_dpp %4, %20, %12, vcc quad_perm:" qp " row_mask:0xf bank_mask:0xf\n\t" \
  "v_cndmask_b32_dpp %5, %21, %13, vcc quad_perm:" qp " row_mask:0xf bank_mask:0xf\n\t" \
  "v_cndmask_b32_dpp %6, %22, %14, vcc quad_perm:" qp " row_mask:0xf bank_mask:0xf\n\t" \
  "v_cndmask_b32_dpp %7, %23, %15, vcc quad_perm:" qp " row_mask:0xf bank_mask:0xf"
#define TSTEP_OPS(o, g0, g1, s, x) \
  : "=&v"(o[g0][0]), "=&v"(o[g0][1]), "=&v"(o[g0][2]), "=&v"(o[g0][3]), "=&v"(o[g1][0]), "=&v"(o[g1][1]), "=&v"(o[g1][2]), "=&v"(o[g1][3]) \
  : "v"(s[g0][0]), "v"(s[g0][1]), "v"(s[g0][2]), "v"(s[g0][3]), "v"(s[g1][0]), "v"(s[g1][1]), "v"(s[g1][2]), "v"(s[g1][3]), \
    "v"(s[(g0) xor (x)][0]), "v"(s[(g0) xor (x)][1]), "v"(s[(g0) xor (x)][2]), "v"(s[(g0) xor (x)][3]), "v"(s[(g1) xor (x)][0]), "v"(s[(g1) xor (x)][1]), "v"(s[(g1) xor (x)][2]), "v"(s[(g1) xor (x)][3])
DEV void tstep(TUnit& U, const u32x4 (&r)[4], int k) {
  if (k == 0) asm volatile(TSTEP_ASM("[1,0,3,2]") TSTEP_OPS(U.t, 0, 2, r, 1), "i"(0x55555555u) : "vcc");
  if (k == 1) asm volatile(TSTEP_ASM("[1,0,3,2]") TSTEP_OPS(U.t, 1, 3, r, 1), "i"(0xAAAAAAAAu) : "vcc");
  if (k == 2) asm volatile(TSTEP_ASM("[2,3,0,1]") TSTEP_OPS(U.o, 0, 1, U.t, 2), "i"(0x33333333u) : "vcc");
  if (k == 3) asm volatile(TSTEP_ASM("[2,3,0,1]") "\n\ts_nop 1" TSTEP_OPS(U.o, 2, 3, U.t, 2), "i"(0xCCCCCCCCu) : "vcc");
}
DEV u32x4 tout(const TUnit& U, int g) { return u32x4{U.o[g][0], U.o[g][1], U.o[g][2], U.o[g][3]}; }

template <int V> struct IC { static constexpr int value = V; };
template <int I, int N, class F> DEV void sfor(F&& f) {
  if constexpr (I < N) { f(IC<I>{}); sfor<I + 1, N>(f); }
}
#define BARRIER() do { WAITL0(); HWBAR(); SCHED(); } while (0)

template <class F>
DEV void kgen(f32x16& kacc, const u32x4 (&praw)[3], const u32 (&px)[6], float ldt, float ldtx, u32 sr, u32 T, int lane, F&& shared_done) {
  const float dt0 = bf2f(pk2(expf(ldt), 0.f) & 0xffffu), dtx = bf2f(pk2(expf(ldtx), 0.f) & 0xffffu);
  const u32 rdp = opq(sr + (u32)((lane & 31) * 272 + (lane >> 5) * 16));
#pragma unroll
  for (int k = 0; k < 3; ++k) *lp<u32x4>(T + 1024 + lane * 16, k * 1024) = praw[k];
  CBAR();
  const u32 prd = opq(T + 1024 + lane * 2);
  m0_set(sr);
  auto put = [&](int row, h2_t v) { asm volatile("ds_write_addtid_b32 %0 offset:%1" ::"v"(v), "i"(row * 272) : "memory"); };
  kacc = f32x16{};
  float ar[4], ai[4], wr[4], wi[4], mx[2] = {0.f, 0.f}, wsc[2], kinv[2];
#pragma unroll
  for (int ps = 0; ps < 4; ++ps) {
    u32 pw[6];
#pragma unroll
    for (int k = 0; k < 6; ++k) pw[k] = ps ? (u32)*lp<u16>(prd, (k >> 1) * 1024 + (k & 1) * 384 + (ps - 1) * 128) : (lane < 48 ? px[k] : 0u);
    const float dt = ps ? dt0 : dtx;
    ar[ps] = (dt * bf2f(pw[0])) * 1.4426950409f;
    ai[ps] = (dt * bf2f(pw[1])) * 0.15915494309f;
    const float br = dt * bf2f(pw[2]), bi = dt * bf2f(pw[3]), cr = bf2f(pw[4]), ci = bf2f(pw[5]);
    wr[ps] = __uint_as_float(opq(__float_as_uint(__builtin_fmaf(cr, br, -ci * bi))));
    wi[ps] = -__builtin_fmaf(cr, bi, ci * br);
    mx[ps == 0] = fmaxf(mx[ps == 0], fmaxf(fabsf(wr[ps]), fabsf(wi[ps])));
  }
#pragma unroll
  for (int k = 0; k < 2; ++k) {
    float m = mx[k];
    m = fmaxf(m, dppf<0x128>(m)); m = fmaxf(m, dppf<0x124>(m)); m = fmaxf(m, dppf<0x122>(m)); m = fmaxf(m, dppf<0x121>(m));
    const u32 mb = __float_as_uint(m);
    const u32 ex = min(max(max(max((u32)__builtin_amdgcn_readlane(mb, 0), (u32)__builtin_amdgcn_readlane(mb, 16)),
                               max((u32)__builtin_amdgcn_readlane(mb, 32), (u32)__builtin_amdgcn_readlane(mb, 48))) >> 23, 1u), 252u);
    wsc[k] = __uint_as_float((253u - ex) << 23);
    kinv[k] = __uint_as_float((ex + 1u) << 23);
  }
  h2_t gw[4];
#pragma unroll
  for (int ps = 0; ps < 4; ++ps) gw[ps] = toh(f32x2{__uint_as_float(opq(__float_as_uint(wr[ps] * wsc[ps == 0]))), wi[ps] * wsc[ps == 0]});
  h2_t c1, c2, c4, c256, c512, c1024, g0, qv[32];
  auto setup = [&]() {
    const f32x2 e1 = cexp2(ar[0], -ai[0], 1.f), e8 = cexp2(ar[0], ai[0], 8.f), e256 = cexp2(ar[0], -ai[0], 256.f);
    const f32x2 e2 = csq(e1), e16 = csq(e8), e512 = csq(e256), e4 = csq(e2), e32 = csq(e16), e1024 = csq(e512), e64 = csq(e32), e128 = csq(e64);
    c1 = toh(e1); c2 = toh(e2); c4 = toh(e4); c256 = toh(e256); c512 = toh(e512); c1024 = toh(e1024);
    qv[1] = toh(e8); qv[2] = toh(e16); qv[4] = toh(e32); qv[8] = toh(e64); qv[16] = toh(e128);
    g0 = gw[0];
#pragma unroll
    for (int k = 0; k < 3; ++k) { ar[k] = ar[k + 1]; ai[k] = ai[k + 1]; gw[k] = gw[k + 1]; }
  };
  qv[0] = h2_t{(_Float16)1.f, (_Float16)0.f};
  setup();
  SCHED();
#pragma unroll
  for (int ps = 0; ps < 4; ++ps) {
    {
      const h2_t p1 = hmul(g0, c1), p2 = hmul(g0, c2), p4 = hmul(g0, c4), g1 = hmul(g0, c256), g2 = hmul(g0, c512), g4 = hmul(g0, c1024);
      const h2_t p3 = hmul(p1, c2), p258 = hmul(g1, c2), g3 = hmul(g1, c512), p5 = hmul(p1, c4);
      SCHED();
      put(0, g0); put(1, p1); put(2, p2); put(4, p4); put(8, g1); put(12, g2); put(16, g4);
      SCHED();
      const h2_t p6 = hmul(p2, c4), p260 = hmul(g1, c4), p516 = hmul(g2, c4), g5 = hmul(g1, c1024), g6 = hmul(g2, c1024), g8 = hmul(g4, c1024);
      const h2_t p7 = hmul(p3, c4), p262 = hmul(p258, c4), p772 = hmul(g3, c4), g7 = hmul(g3, c1024);
      SCHED();
      put(3, p3); put(9, p258); put(14, g3); put(5, p5); put(6, p6); put(10, p260); put(13, p516);
      SCHED();
      qv[3] = hmul(qv[1], qv[2]); qv[5] = hmul(qv[1], qv[4]); qv[6] = hmul(qv[2], qv[4]); qv[24] = hmul(qv[8], qv[16]);
      qv[9] = hmul(qv[1], qv[8]); qv[10] = hmul(qv[2], qv[8]); qv[12] = hmul(qv[4], qv[8]); qv[7] = hmul(qv[3], qv[4]);
      SCHED();
      put(17, g5); put(18, g6); put(20, g8); put(7, p7); put(11, p262); put(15, p772); put(19, g7);
    }
    h8_t pf[8], qf[8];
#pragma unroll
    for (int kk = 0; kk < 8; ++kk) pf[kk] = *lp<h8_t>(rdp, kk * 32);
    CBAR();
    SCHED();
    qv[11] = hmul(qv[3], qv[8]); qv[13] = hmul(qv[5], qv[8]); qv[14] = hmul(qv[6], qv[8]); qv[15] = hmul(qv[7], qv[8]);
    SCHED();
#pragma unroll
    for (int j = 0; j < 8; ++j) put(j, qv[j]);
    SCHED();
#pragma unroll
    for (int j = 1; j < 8; ++j) qv[16 + j] = hmul(qv[j], qv[16]);
    SCHED();
#pragma unroll
    for (int j = 8; j < 16; ++j) put(j, qv[j]);
    SCHED();
#pragma unroll
    for (int j = 1; j < 8; ++j) qv[24 + j] = hmul(qv[j], qv[24]);
    SCHED();
#pragma unroll
    for (int j = 16; j < 32; ++j) put(j, qv[j]);
#pragma unroll
    for (int kk = 0; kk < 8; ++kk) qf[kk] = *lp<h8_t>(rdp, kk * 32);
    CBAR();
    SCHED();
    if (ps == 1) {
#pragma unroll
      for (int r = 0; r < 12; ++r) kacc[r] = __uint_as_float(opq(__float_as_uint(kacc[r] * kinv[1])));
      shared_done(kacc);
      kacc = f32x16{};
    }
    SCHED();
    if (ps < 3) setup();
#pragma unroll
    for (int kk = 0; kk < 8; ++kk) kacc = MFMA32H(pf[kk], qf[kk], kacc);
    __builtin_amdgcn_sched_group_barrier(0x402, 20, 0);
#pragma unroll
    for (int kk = 0; kk < 8; ++kk) { __builtin_amdgcn_sched_group_barrier(0x8, 1, 0); __builtin_amdgcn_sched_group_barrier(0x402, 5, 0); }
    SCHED();
  }
#pragma unroll
  for (int r = 0; r < 12; ++r) kacc[r] = __uint_as_float(opq(__float_as_uint(kacc[r] * kinv[0])));
}
DEV void expand(f32x16 kacc, float dsk, u32 CB, u32 T, int lane) {
  const int bq = lane & 31, hh = lane >> 5;
  kacc[0] += __uint_as_float(opq(lane == 0 ? __float_as_uint(dsk) : 0u));
  *lp<u32x2>(T + lane * 8) = u32x2{0u, 0u};
  *lp<u32x2>(T + 512 + (u32)(bq * 16 + hh * 8)) = u32x2{pk2(kacc[0], kacc[1]), pk2(kacc[2], kacc[3])};
  *lp<u32>(CB + (u32)(hh ? 256 + 4 * bq : 8 * bq)) = pk2(kacc[4], kacc[5]);
  *lp<u32>(CB + (u32)(hh ? 384 + 4 * bq : 8 * bq + 4)) = pk2(kacc[6], kacc[7]);
#pragma unroll
  for (int r = 0; r < 4; ++r) *lp<u16>(CB + (u32)(512 + 64 * (4 * hh + r) + 2 * bq)) = (u16)pk2(kacc[8 + r], 0.f);
  CBAR();
  {
    const u32x2 v = *lp<u32x2>(CB + (u32)(8 * bq));
    const u32 n4 = *lp<u16>(CB + (u32)(8 * bq + 8));
    const u32x4 o = {__builtin_amdgcn_perm(v.x, v.x, 0x01000100u), __builtin_amdgcn_perm(v.y, v.x, 0x05040302u),
                     __builtin_amdgcn_perm(v.y, v.y, 0x01000100u), __builtin_amdgcn_perm(n4, v.y, 0x05040302u)};
    *lp<u32x4>(T + 1024 + (u32)(bq * 16)) = o;
  }
  {
    const u32 v = *lp<u32>(CB + (u32)(256 + 4 * lane));
    const u32 n2 = *lp<u16>(CB + (u32)(256 + 4 * lane + 4));
    *lp<u32x4>(T + 1536 + (u32)(lane * 16)) = u32x4{__builtin_amdgcn_perm(v, v, 0x01000100u), v, __builtin_amdgcn_perm(v, v, 0x03020302u), n2 * 0x10001u};
  }
#pragma unroll
  for (int g = 0; g < 2; ++g) {
    const u32 v0 = *lp<u16>(CB + (u32)(512 + 2 * (g * 64 + lane))), v1 = *lp<u16>(CB + (u32)(514 + 2 * (g * 64 + lane)));
    *lp<u32x4>(T + 2560 + (u32)((g * 64 + lane) * 16)) = u32x4{v0 * 0x10001u, v0 * 0x10001u, ((lane & 1) ? v1 : v0) | (v1 << 16), v1 * 0x10001u};
  }
  CBAR();
}
DEV void prm_load(const Params* kp, u32x4 (&praw)[3], u32& ldt, u32& dsk, int chn, int lane) {
  const int l24 = lane < 24 ? lane : (lane < 48 ? lane - 24 : 0);
  const size_t po = (size_t)chn * NST + l24 * 8;
  praw[0] = *(const u32x4*)((lane < 24 ? kp->Are : kp->Aim) + po);
  praw[1] = *(const u32x4*)((lane < 24 ? kp->Bre : kp->Bim) + po);
  praw[2] = *(const u32x4*)((lane < 24 ? kp->Cre : kp->Cim) + po);
  ldt = kp->logdt[chn];
  dsk = kp->dskip[chn];
}
DEV void u_load(u32x4 (&d)[4], char* ws, int chn, int lane) {
  const __amdgpu_buffer_rsrc_t r_ws = mk_rsrc(ws, WS_END);
#pragma unroll
  for (int g = 0; g < 4; ++g) d[g] = __builtin_amdgcn_raw_buffer_load_b128(r_ws, WS_UT + (u32)(chn * 4096 + lane * 16), g * 1024, 16);
  CBAR();
}
DEV u32 part_base(int w) { return w < 4 ? w * SC_SZ + 1024 : (w < 7 ? L_T + 8 * T_SZ + (w - 4) * 3072 : L_UA + 10 * U_SZ); }
static_assert(L_UA + 10 * U_SZ + 3072 <= L_GATE, "lds");
DEV void u_put(const u32x4 (&d)[4], int k, int lane) {
  const u32 ra = opq(L_UA + k * U_SZ + lane * 16), wb = opq(ub_base(k) + lane * 16);
  u32 n[4];
  *lp<u32>((lane < 32 ? L_UA + k * U_SZ : ub_base(k)) + 4096 + (lane & 15) * 4) = 0u;
#pragma unroll
  for (int g = 0; g < 4; ++g) *lp<u32x4>(ra, g * 1024) = d[g];
  CBAR();
#pragma unroll
  for (int g = 0; g < 4; ++g) n[g] = *lp<u32>(ra, g * 1024 + 16);
#pragma unroll
  for (int g = 0; g < 4; ++g)
    *lp<u32x4>(wb, g * 1024) = u32x4{__builtin_amdgcn_alignbit(d[g].y, d[g].x, 16), __builtin_amdgcn_alignbit(d[g].z, d[g].y, 16),
                                     __builtin_amdgcn_alignbit(d[g].w, d[g].z, 16), __builtin_amdgcn_alignbit(n[g], d[g].w, 16)};
  CBAR();
}
template <int TM> DEV void conv(char* ws, int k, int chn, u32 tag, int sidx, int lane) {
  constexpr int NT = TM >= 64 ? 8 : 6;
  const int c16 = lane & 15, q = lane >> 4;
  u32 ab[4], bb = opq(L_T + k * T_SZ + (u32)(32 * c16 + 16 * q + 480));
#pragma unroll
  for (int h = 0; h < 4; ++h) ab[h] = opq(((c16 & 1) ? L_UA + k * U_SZ : ub_base(k)) + (u32)(3548 - 1024 * h - 4 * (c16 >> 1) + 16 * q));
  auto ldA = [&](int p) {
    const u32 o = (p & 1) ? 0 : 512;
    return u32x4{*lp<u32>(ab[p >> 1], o), *lp<u32>(ab[p >> 1], o + 4), *lp<u32>(ab[p >> 1], o + 8), *lp<u32>(ab[p >> 1], o + 12)};
  };
  f32x4 y[8] = {};
#pragma unroll
  for (int c = 0; c < 8; ++c) asm volatile("" : "+v"(y[c]));
  u32x4 fa[8];
  bf16x8 fb[8];
#pragma unroll
  for (int p = NT - 1; p >= 0; --p) { fa[p] = ldA(p); fb[NT - 1 - p] = lds128(bb, 512 * (NT - 1 - p)); }
#pragma nounroll
  for (int r = 0; r < 8; ++r) {
    bb -= 64;
#pragma unroll
    for (int h = 0; h < 4; ++h) ab[h] -= 64;
    SCHED();
#pragma unroll
    for (int p = NT - 1; p >= 0; --p) {
#pragma unroll
      for (int m = 0; m + p < NT; ++m) {
        if ((TM >> (p + m)) & 1) mfma16_ip(y[p + m], as8(fa[p]), fb[m]);
        if (p == 0) { SCHED(); fb[m] = lds128(bb, 512 * m); SCHED(); }
      }
      SCHED();
      fa[p] = ldA(p);
      SCHED();
    }
  }
  asm volatile("s_nop 7" : "+v"(y[0]), "+v"(y[1]), "+v"(y[2]), "+v"(y[3]), "+v"(y[4]), "+v"(y[5]), "+v"(y[6]), "+v"(y[7]));
  const __amdgpu_buffer_rsrc_t r_ws = mk_rsrc(ws, WS_END);
  const u32 ao = WS_A + (u32)(chn * 4096 + (q & 1) * 512 + c16 * 32 + (q >> 1) * 16);
#pragma unroll
  for (int c = 0; c < 8; c += 2) {
    if ((TM >> c) & 1) {
      const auto s0 = __builtin_amdgcn_permlane16_swap(pk2(y[c][0], y[c][1]), pk2(y[c + 1][0], y[c + 1][1]), false, false);
      const auto s1 = __builtin_amdgcn_permlane16_swap(pk2(y[c][2], y[c][3]), pk2(y[c + 1][2], y[c + 1][3]), false, false);
      __builtin_amdgcn_raw_buffer_store_b128(u32x4{s0[0], s1[0], s0[1], s1[1]}, r_ws, ao + c * 512, 0, 16);
    }
  }
}
#define PFA 1
template <int N> DEV void pf_wsp(const u16* w_sp, int sidx, int w4, int lane, int k0) {
  m0_set(L_GATE);
  const u32 vo = (u32)((sidx >> 4) * 512 * 4096 + (((sidx & 7) * 4 + w4) * 64 + lane) * 128);
#pragma unroll
  for (int k = 0; k < N; ++k)
    asm volatile("s_nop 0\n\tbuffer_load_dword %0, %1, %2 offen lds" ::"v"(vo), "s"(mk_srd(w_sp, SEQ * SEQ * 2)), "s"((u32)(k0 + k) * 262144u) : "memory");
}
DEV void ssm_wg(int sidx, int w, int lane TSARG) {
  Params P;
  {
    const KP k0 = kargs();
    P.u = k0->u; P.Are = k0->Are; P.Aim = k0->Aim; P.Bre = k0->Bre; P.Bim = k0->Bim; P.Cre = k0->Cre; P.Cim = k0->Cim; P.logdt = k0->logdt;
    P.dskip = k0->dskip; P.w_sp = k0->w_sp; P.ws = k0->ws;
  }
  char* ws = P.ws;
  const int c0 = 10 * sidx, cx = c0 + 8 + (w & 1), c16 = lane & 15, q = lane >> 4;
  const Params* kp = &P;
  const u32x4 s_ws = mk_srd(ws, WS_END);
  const u32 sc = L_UA + (w & 3) * 10240;
  const int trb = (sidx >> 4) * 8 + (sidx & 7), thf = (sidx >> 3) & 1, tch = 320 * thf + 80 * (w & 3);
  u32x4 praw[3];
  u32 ldt, dsk, px[6];
  prm_load(kp, praw, ldt, dsk, c0 + w, lane);
  {
    const int o = cx * NST + 48 * (w >> 1) + min(lane, 47);
    px[0] = kp->Are[o]; px[1] = kp->Aim[o]; px[2] = kp->Bre[o]; px[3] = kp->Bim[o]; px[4] = kp->Cre[o]; px[5] = kp->Cim[o];
  }
  const u32 ldtx = kp->logdt[cx], dskx = kp->dskip[c0 + 8 + (w & 1)];
  if (w >= 4) {
    const u32x4 s_x = mk_srd(kp->u, SEQ * DM * 2);
#pragma unroll
    for (int u = 0; u < 5; ++u) {
      const int off = u * 1024 + lane * 16;
      const u32 vo = (u32)((off / 160) * 1280 + (off % 160) + trb * 64 * 1280 + tch * 2);
      dma16(s_x, vo, 0, sc + u * 1024);
      dma16(s_x, vo, 32 * 1280, sc + 5120 + u * 1024);
    }
  }
  const u32 ep_raw = __builtin_amdgcn_raw_buffer_load_b32(mk_rsrc(ws, WS_END), 0, 0, 16);
  BARRIER();
  if (w >= 4) {
    __builtin_amdgcn_s_setprio(3);
    __builtin_amdgcn_s_waitcnt(0x0F70);
    const u32 hr = opq(sc + (u32)((8 * q + 3 - ((lane >> 2) & 3)) * 160 + 8 * (lane & 3)));
    bf16x8 tv[2][5];
#pragma unroll
    for (int g = 0; g < 2; ++g)
#pragma unroll
      for (int e = 0; e < 5; ++e) tv[g][e] = cat8(ldstr(hr, g * 5120 + 640 + 32 * e), ldstr(hr, g * 5120 + 32 * e));
    CBAR();
#pragma unroll
    for (int g = 0; g < 2; ++g) {
      const u32 tw = opq(sc + (u32)(c16 * 128 + (((7 - 4 * g - q) ^ (c16 & 7)) * 16)));
#pragma unroll
      for (int e = 0; e < 5; ++e) *lp<bf16x8>(tw, e * 2048) = tv[g][e];
    }
    CBAR();
    const __amdgpu_buffer_rsrc_t r_ws = mk_rsrc(ws, WS_END);
    const u32 tr = opq(sc + (u32)((lane >> 3) * 128 + (((lane & 7) ^ (lane >> 3)) * 16)));
    const u32 uo = WS_UT + (u32)((tch + (lane >> 3)) * 4096 + (248 - 8 * trb + (lane & 7)) * 16);
#pragma unroll
    for (int k = 0; k < 10; ++k) __builtin_amdgcn_raw_buffer_store_b128(*lp<u32x4>(tr, k * 1024), r_ws, uo + (u32)(k * 8 * 4096), 0, 16);
    pf_wsp<PFA>(kp->w_sp, sidx, w & 3, lane, (sidx & 8) / 2);
    WAITV(PFA);
    gflag_st_last(faddr(F_UC), 3, s_ws, WS_FUT + (u32)((2 * trb + thf) * 256), __builtin_amdgcn_readfirstlane(ep_raw) + 1u);
    __builtin_amdgcn_s_setprio(1);
    pf_wsp<4 - PFA>(kp->w_sp, sidx, w & 3, lane, (sidx & 8) / 2 + PFA);
  } else {
    __builtin_amdgcn_s_setprio(2);
    __builtin_amdgcn_s_waitcnt(0x0F70);
  }
  const u32 tag = __builtin_amdgcn_readfirstlane(ep_raw) + 1u;
  STAMP(1);
  f32x16 kacc;
  f32x4 kp4[3];
  u32x4 ud[4], ux[4];
  auto part_put = [&]() {
#pragma unroll
    for (int k = 0; k < 3; ++k) *lp<f32x4>(part_base(w) + lane * 16, k * 1024) = kp4[k];
    flag_inc(faddr(F_PC));
  };
  kgen(kacc, praw, px, bf2f(ldt), bf2f(ldtx), w * SC_SZ, L_T + w * T_SZ, lane, [&](const f32x16& kx) {
    kp4[0] = f32x4{kx[0], kx[1], kx[2], kx[3]}; kp4[1] = f32x4{kx[4], kx[5], kx[6], kx[7]}; kp4[2] = f32x4{kx[8], kx[9], kx[10], kx[11]};
    if (w >= 4) part_put();
  });
  gflag_wait(s_ws, WS_FUT + (u32)((2 * (lane & 31) + (sidx >> 5)) * 256), tag);
  u_load(ud, ws, c0 + w, lane);
  u_load(ux, ws, cx, lane);
  if (w < 4) part_put();
  expand(kacc, bf2f(dsk), w * SC_SZ, L_T + w * T_SZ, lane);
  STAMP(2);
  flag_wait<2>(faddr(F_UC), 4);
  u_put(ud, w, lane);
  STAMP(3);
  if (w < 2) {
    flag_wait<2>(faddr(F_PC), 8);
#pragma unroll
    for (int k = 0; k < 3; ++k) {
      const u32 o = (u32)(k * 1024 + lane * 16);
      const f32x4 v = (*lp<f32x4>(part_base(w) + o) + *lp<f32x4>(part_base(w + 2) + o)) + (*lp<f32x4>(part_base(w + 4) + o) + *lp<f32x4>(part_base(w + 6) + o));
#pragma unroll
      for (int r = 0; r < 4; ++r) kacc[4 * k + r] = v[r];
    }
    flag_inc(faddr(F_PC));
    flag_wait<1>(faddr(F_PC), 10);
    expand(kacc, bf2f(dskx), w * SC_SZ, L_T + (w + 8) * T_SZ, lane);
    u_put(ux, w + 8, lane);
    flag_inc(faddr(F_XT));
  }
  __builtin_amdgcn_s_setprio(0);
  u32 pfw[3] = {};
    {
      const u32x4 s_p = mk_srd(kargs()->w_out, w < 4 ? FH * DM * 2 : 0);
      const u32 ln = (u32)(((sidx & 7) * 4 + (w & 3)) * 48 + min(lane, 47)), vo = (ln / 6) * 1280 + (ln % 6) * 128 + ((sidx >> 3) & 1) * 512;
      asm volatile("buffer_load_dword %0, %3, %4, 0 offen\n\tbuffer_load_dword %1, %3, %4, %5 offen\n\tbuffer_load_dword %2, %3, %4, %6 offen"
                   : "=&v"(pfw[0]), "=&v"(pfw[1]), "=&v"(pfw[2]) : "v"(vo), "s"(s_p), "s"(256u * 1280u), "s"(512u * 1280u) : "memory");
    }
  STAMP(4);
  conv<0xFF>(ws, w, c0 + w, tag, sidx, lane);
  STAMP(5);
  if (w < 4) {
    flag_wait<4>(faddr(F_XT), 2);
    if (w & 1) conv<0xC3>(ws, 8 + (w >> 1), c0 + 8 + (w >> 1), tag, sidx, lane);
    else conv<0x3C>(ws, 8 + (w >> 1), c0 + 8 + (w >> 1), tag, sidx, lane);
  }
  WAITV(0);
  asm volatile("" ::"v"(pfw[0]), "v"(pfw[1]), "v"(pfw[2]));
  gflag_st_last(faddr(F_ACNT), 7, s_ws, WS_FA + (u32)(sidx * 256), tag);
  STAMP(6);
  TSFLUSH();
}

DEV void a_load(u32x4 (&R)[4][4], u32x4 srd, u32 voff, u32 sbase, int n) {
  bload(R[n >> 2][n & 3], srd, voff, sbase + (u32)((32 * (n >> 3) + 4 * ((n >> 2) & 1) + (n & 3)) * 4096));
}
DEV void a_take(bf16x8 (&A)[8][4], const TUnit& U, int un) {
#pragma unroll
  for (int g = 0; g < 4; ++g) A[4 * (un >> 2) + g][un & 3] = as8(tout(U, g));
}
#define NPREV 12
constexpr int NPRE = NPREV;
#define POLL_IT 14
#define FLAG_IT (POLL_IT + 3)
constexpr int na(int i) { return i == 9 ? 10 : (i >= 10 && i <= 15) ? 1 : 0; }
constexpr int TU0 = 12;
constexpr int nd(int i) { return (i >= 10 && i <= 17 ? 1 : 0) + (i >= 16 ? 2 : 0); }
constexpr int a_first(int i) { return i <= 0 ? 0 : a_first(i - 1) + na(i - 1); }
constexpr int n_lead(int i) { return i == POLL_IT - 2 ? 1 : 0; }
constexpr int n_trail(int i) { return i == POLL_IT + 1 ? 2 : 0; }
constexpr int n_dma(int i) { return i < 6 ? 3 : i + 4 < 20 ? 2 : 0; }
constexpr int NP2 = 12;
constexpr int issued(int it) { return it <= 0 ? NP2 : issued(it - 1) + n_lead(it - 1) + n_dma(it - 1) + na(it - 1) + nd(it - 1) + n_trail(it - 1); }
constexpr int w_end(int c) { return c < 4 ? 3 * (c + 1) : issued(c - 4) + n_lead(c - 4) + n_dma(c - 4); }
constexpr int a_end(int n) {
  int i = 0;
  while (a_first(i + 1) <= n) ++i;
  return issued(i) + n_lead(i) + n_dma(i) + n - a_first(i) + 1;
}
constexpr int imin(int a, int b) { return a < b ? a : b; }
static_assert(a_first(20) == 16, "schedule");
#define WINSZ 8
constexpr int WIN = WINSZ;
static_assert(WIN <= 13, "h must be older than the loads waited for at its flag");
constexpr int N_POLL = 46, N_H0 = 58;
constexpr bool wo_at(int n) { return n >= 52 && n < 63; }
constexpr int z_younger(int n) {
  int c = (n + WIN - 1 < 63 ? n + WIN - 1 : 63) - n + (n > N_POLL && n <= N_POLL + WIN ? 1 : 0) + (n > N_H0 ? n - N_H0 : 0);
  for (int k = n - WIN; k < n; ++k) c += wo_at(k);
  return c;
}
static_assert(N_H0 == 58 && N_H0 + WIN > 63 && N_POLL + WIN < 64, "schedule");

DEV void wo_piece(u32x4 s_wo, int lane, int tj, int rot4, int pl, int jj, int u, u32 dst) {
  const int c16 = lane & 15, q = lane >> 4;
  dma16(s_wo, (u32)(q * 1280 + tj * 256 + (c16 ^ (2 * (q + 4 * (u & 1)))) * 16), (u32)((128 * jj + 32 * ((pl + rot4) & 3) + 4 * u) * 1280), dst + u * 1024);
}
#define H3_AT 6
DEV void tail3(char* ws, u32 tag, int ti, int tj, int w, int lane, u32x4 (&h0)[6] TSARG) {
  const int c16 = lane & 15, q = lane >> 4, a4 = (lane >> 2) & 3, p4 = lane & 3, rot4 = ROT4EXPR;
  const __amdgpu_buffer_rsrc_t r_ws = mk_rsrc(ws, WS_END);
  const u32x4 s_ws = mk_srd(ws, WS_END), s_wo = mk_srd(kargs()->w_out, FH * DM * 2);
  if (tj == 5) {
  } else if (w >= 4) {
    const u32 lv0 = (u32)((8 * (w - 4) + q) * 1280 + tj * 256 + (c16 ^ (2 * q)) * 16), lv1 = (lv0 ^ 128) + 4 * 1280 - 1024;
    auto blk = [&](int pl, int jj, u32 dst) {
      m0_set(dst + (w - 4) * 2048);
      u32 so = (u32)((128 * jj + 32 * ((pl + rot4) & 3)) * 1280);
      asm volatile("s_nop 0" : "+s"(so));
      dma16o<0>(s_wo, lv0, so);
      dma16o<1024>(s_wo, lv1, so);
    };
#pragma unroll
    for (int k = 0; k < 6; ++k) {
      if (k == 3) { WAITV(7); HWBAR(); }
      blk(2, (k >> 1) + 3 * (k & 1), L_W2 + ((k >> 1) + 3 * (k & 1)) * 8192);
    }
    u32 av_p;
    const u32 fa = WS_FA + (u32)(min((128 * tj + 32 * (w - 4)) / 10 + (lane & 7), (128 * tj + 32 * (w - 4) + 31) / 10) * 256);
    asm volatile("buffer_load_dword %0, %1, %2, 0 offen sc1" : "=v"(av_p) : "v"(fa), "s"(s_ws) : "memory");
#pragma unroll
    for (int k = 0; k < 6; ++k) {
      if (k == 3) { WAITV(7); HWBAR(); }
      blk(3, (k >> 1) + 3 * (k & 1), L_W3 + ((k >> 1) + 3 * (k & 1)) * 8192);
    }
    WAITV(12);
    SCHED();
    const bool early = __all(av_p == tag);
    u32x4 av[4];
    auto a_load4 = [&]() {
#pragma unroll
      for (int k = 0; k < 4; ++k)
        av[k] = __builtin_amdgcn_raw_buffer_load_b128(r_ws, WS_A + (u32)((tj * 128 + 32 * (w - 4) + (lane >> 3)) * 4096 + ti * 128 + (lane & 7) * 16), k * 8 * 4096, 16);
      CBAR();
    };
    if (early) { a_load4(); WAITV(4); } else WAITV(0);
    HWBAR();
    if (!early) { gflag_wait(s_ws, fa, tag); a_load4(); }
    const u32 aw = opq(L_AT + (u32)((32 * (w - 4) + (lane >> 3)) * 128 + (((lane & 7) ^ (lane >> 3)) * 16)));
#pragma unroll
    for (int k = 0; k < 4; ++k) *lp<u32x4>(aw, k * 1024) = av[k];
    STAMP(10);
    BARRIER();
    HWBAR();
    if (blockIdx.x == 64 && w == 7) {
      gflag_wait(s_ws, WS_FA + (u32)(lane * 256), tag);
      gflag_st(s_ws, WS_EPOCH, tag);
    }
  } else {
    const int kk = w >> 1, mt = 2 * (w & 1) + kk;
    const u32 hb = WS_H + (u32)(ti * 4 * 24576 + kk * 3072 + lane * 16);
    u32x4 hf[4][6];
    auto hload = [&](int p, int j0 = 0, int j1 = 6) {
#pragma unroll
      for (int j = j0; j < j1; ++j) hf[p][j] = __builtin_amdgcn_raw_buffer_load_b128(r_ws, hb, (u32)((mt ^ (j & 1)) * 24576 + p * 6144 + (j >> 1) * 1024), 16);
    };
    auto hcheck = [&](int p) {
      auto bad = [&]() {
        u32 m = 0;
#pragma unroll
        for (int j = 0; j < 6; ++j) m = max(max(m, max(hf[p][j].x, hf[p][j].y)), max(hf[p][j].z, hf[p][j].w));
        return __any(m == 0xFFFFFFFFu);
      };
      if (__builtin_expect(bad(), 0)) {
        u32 sp = 0;
#pragma nounroll
        do {
          __builtin_amdgcn_s_sleep(2);
          CBAR();
          hload(p);
        } while (bad() && ++sp < 0x4000u);
      }
      asm volatile("s_nop 1");
    };

    f32x4 o[2][8];
    const u32 wrd = (u32)((4 * q + a4) * 256 + (((4 * q + a4) & 7) * 32) + 8 * p4);
    u32 ad[3][8];
#pragma unroll
    for (int e = 0; e < 8; ++e) {
      ad[0][e] = opq((wrd ^ (u32)(32 * e)) + L_W0 + kk * 24576);
      ad[1][e] = opq((wrd ^ (u32)(32 * e)) + L_W2 + kk * 24576);
      ad[2][e] = opq((wrd ^ (u32)(32 * e)) + (kk ? L_W1B : L_W1 + 16384));
    }
    s16x4 wh[2][8][2];
    auto w_rd = [&](int n, int k) {
      const int p = n / 3, i = n % 3;
      const u32 off = n == 5 ? 0u : (u32)((p == 1 ? L_W1 - L_W0 : 0) + i * 8192);
      wh[n & 1][k >> 1][k & 1] = ldstr(ad[n == 5 ? 2 : p == 2 ? 1 : 0][k >> 1], off + (k & 1) * 4096);
    };
#pragma unroll
    for (int k = 0; k < 16; ++k) w_rd(0, k);
#pragma unroll
    for (int j = 0; j < 6; ++j) hf[0][j] = h0[j];
    STAMPS(3, 1);
    u32x2 at[8];
    float gate;
    const __amdgpu_buffer_rsrc_t r_out = mk_rsrc(kargs()->out, SEQ * DM * 2);
    sfor<0, 12>([&](auto IN) {
      constexpr int n = decltype(IN)::value;
      if (n == 11) {
        HWBAR();
        {
          const u32 ab = opq(L_AT + (u32)((4 * q + a4) * 128 + (((4 * (mt >> 1) + p4) ^ ((4 * q + a4) & 7)) * 16) + 8 * (mt & 1)));
    #pragma unroll
          for (int e = 0; e < 8; ++e) at[e] = __builtin_bit_cast(u32x2, ldstr(ab, 2048 * e));
        }
        gate = *lp<float>(L_GATE + (u32)((mt * 16 + c16) * 4));
      }
      if (n == 2) WAITV(7);
      if (n == 2 || n == 5 || n == 8) { __builtin_amdgcn_s_waitcnt(0xC07F); HWBAR(); STAMPS(3, n == 2 ? 2 : n == 5 ? 3 : 4); }
      if (n == 3) hcheck(1);
      if (n == 6) hcheck(2);
      if (n == 9) { hcheck(3); STAMPS(3, 5); }
      SCHED();
#pragma unroll
      for (int k = 0; k < 16; ++k) {
        const int m = n == 11 ? k < 8 : k & 1, e = n == 11 ? k & 7 : k >> 1;
        if (n) mfma16_ip(o[m][e], cat8(wh[n & 1][e][0], wh[n & 1][e][1]), as8(hf[n / 3][2 * (n % 3) + m]));
        else mfma16_z(o[m][e], cat8(wh[0][e][0], wh[0][e][1]), as8(hf[0][m]));
        SCHED();
        if (n < 11) w_rd(n + 1, k);
        if ((n == 0 || n == 2 || n == H3_AT) && (k & 1) && k < 12) { hload(n == 0 ? 1 : n == 2 ? 2 : 3, k >> 1, (k >> 1) + 1); CBAR(); }
        if (n == 11 && k >= 8) *lp<f32x4>(L_W2 + w * 8192 + lane * 16, e * 1024) = o[1][e];
        SCHED();
      }
    });
    STAMPS(3, 6);
    asm volatile("s_nop 6" : "+v"(o[0][4]), "+v"(o[0][5]), "+v"(o[0][6]), "+v"(o[0][7]));
    f32x4 ga[8];
#pragma unroll
    for (int e = 0; e < 8; ++e) {
      ga[e] = __builtin_elementwise_fma(f32x4{gate, gate, gate, gate}, f32x4{bf2f(at[e].x & 0xffffu), bfhi(at[e].x), bf2f(at[e].y & 0xffffu), bfhi(at[e].y)}, o[0][e]);
      asm volatile("" : "+v"(ga[e]));
    }
    BARRIER();
    STAMPS(3, 7);
    const u32 ot = L_W2 + (w ^ 2) * 8192;
#pragma unroll
    for (int e = 0; e < 8; ++e) ga[e] += *lp<f32x4>(ot + lane * 16, e * 1024);
    CBAR();
    const u32 ob = ot + (u32)(c16 * 256 + 8 * (q & 1));
#pragma unroll
    for (int e = 0; e < 8; ++e)
      *lp<u32x2>(ob + (u32)(((2 * e + (q >> 1)) ^ c16) * 16)) =
          u32x2{pk2(ga[e][0], ga[e][1]), pk2(ga[e][2], ga[e][3])};
    CBAR();
#pragma unroll
    for (int k = 0; k < 4; ++k) {
      const u32x4 v = *lp<u32x4>(ot + (u32)((4 * k + q) * 256 + ((c16 ^ (4 * k + q)) * 16)));
      __builtin_amdgcn_raw_buffer_store_b128(v, r_out, (u32)((ti * 64 + 32 * (mt >> 1) + 8 * k + 4 * (mt & 1) + q) * 1280 + tj * 256 + c16 * 16), 0, 0);
    }
    STAMP(11);
    STAMPS(3, 11);
  }
  TSFLUSH();
}

DEV void gemm_wg(const Params& P, int ti, int tj, int w, int lane TSARG) {
  char* ws = P.ws;
  const int c16 = lane & 15, q = lane >> 4, a4 = (lane >> 2) & 3, p4 = lane & 3, mh = w & 1, nq = w >> 1;
  const __amdgpu_buffer_rsrc_t r_ws = mk_rsrc(ws, WS_END);
  const u32x4 s_ws = mk_srd(ws, WS_END);
  const Params* kp = &P;
  const u32x4 s_x = mk_srd(kp->u, SEQ * DM * 2), s_w = mk_srd(kp->w_in, DM * FF * 2), s_wsp = mk_srd(kp->w_sp, SEQ * SEQ * 2);
  const int rot = ROTEXPR, rot4 = ROT4EXPR;
  auto line_of = [&](int l) { const int r = l + rot; return (u32)(r >= 10 ? r - 10 : r); };
  const int xr = 8 * (w & 1) + (lane >> 3);
  const u32 x_v = (u32)((ti * 64 + 32 * (w >> 2) + 8 * (xr >> 2) + 4 * ((w >> 1) & 1) + (xr & 3)) * 1280 + (((lane & 7) ^ (xr >> 1)) * 16));
  const u32 w_v0 = (u32)((8 * w + q) * 3072 + ((c16 ^ (2 * (q + 4 * (w & 1)))) * 16) + tj * 256), w_v1 = w_v0 + 4 * 3072 - 1024;
  auto x_issue = [&](int l) { dma16(s_x, x_v, line_of(l) * 128, L_X + l * 8192 + w * 1024); };
  auto w_issue = [&](int c) {
    m0_set(L_RING + (c & 3) * CHUNK + w * 2048);
    u32 so = line_of(c % 10) * (64 * 3072) + (c < 10 ? FH * 2 : 0);
    asm volatile("s_nop 0" : "+s"(so));
    dma16o<0>(s_w, w_v0, so);
    dma16o<1024>(s_w, w_v1, so);
  };
  const u32 trow = (u32)(32 * mh + 8 * (c16 >> 2) + (c16 & 3));
  x_issue(0);
  w_issue(0);
  u32 bsp_raw, ep_raw;
  asm volatile("buffer_load_dword %0, off, %1, 0 sc1" : "=v"(ep_raw) : "s"(s_ws) : "memory");
  {
    asm volatile("buffer_load_ushort %0, %1, %2, 0 offen" : "=v"(bsp_raw)
                 : "v"((u32)((ti * 64 + 32 * (w >> 2) + 8 * (c16 >> 2) + 4 * ((w >> 1) & 1) + (c16 & 3)) * 2)), "s"(mk_srd(kp->b_sp, SEQ * 2)) : "memory");
    if (w < 2) dma16(mk_srd(kp->gate_w, DM * 2), lane * 16, w * 256, L_GW + w * 256);
  }
#pragma unroll
  for (int l = 1; l < NPRE / 3; ++l) { x_issue(l); w_issue(l); }
  STAMP(1);
  const u32 rd0 = L_X + (u32)(mh * 4096 + (c16 >> 3) * 1024 + (c16 & 7) * 128 + ((q ^ (c16 >> 1)) * 16));
  const u32 xb[2][2] = {{opq(rd0), opq(rd0 ^ 64)}, {opq(rd0 + 40960), opq((rd0 ^ 64) + 40960)}};
  const u32 wrow = L_RING + (u32)((8 * q + a4) * 256 + 8 * p4), wf = (u32)(a4 + 4 * (q & 1));
  u32 wb[2] = {opq(wrow + ((2 * nq) ^ wf) * 32), opq(wrow + ((2 * nq + 1) ^ wf) * 32)};
  const u32 a_v = (u32)(8 * a4 * 4096 + (4 * q + p4) * 16);
  const int ksl = (w + ti + tj) & 7;
  const u32 a_sb = (u32)(ti * 64 * 4096 + ksl * 512 + (tj & 1) * 256);
  const u32 d_v0 = (u32)((8 * (lane >> 5) + ((lane >> 3) & 3)) * 4096 + (((lane & 7) ^ (lane >> 4)) * 16)), d_v1 = (d_v0 ^ 64) + 16 * 4096;
  bf16x8 Gf[2][2];
  const u32 gxb = opq((rd0 ^ (u32)((nq >> 1) * 64)) + (nq & 1) * 2048), gwb = opq(L_GW + (u32)(q * 16 + (nq >> 1) * 64));
  auto g_read = [&](int c) { Gf[c & 1][0] = lds128(gwb + line_of(c - 10) * 128); Gf[c & 1][1] = lds128(c < 18 ? gxb : gxb + 65536, ((c - 10) & 7) * 8192); };
  bf16x8 Xf[2][2][2];
  s16x4 Wh[2][2][2][2];
  auto f_read = [&](int c, int k) {
    const int l = c % 10;
    if (k < 4) Xf[c & 1][k >> 1][k & 1] = lds128(xb[l >= 5][k >> 1], (l % 5) * 8192 + (k & 1) * 2048);
    else { const int j = k - 4; Wh[c & 1][j >> 2][(j >> 1) & 1][j & 1] = ldstr(wb[(j >> 1) & 1], (c & 3) * CHUNK + (j >> 2) * 8192 + (j & 1) * 1024); }
  };
  auto Wf = [&](int c, int s, int e) { return cat8(Wh[c & 1][s][e][0], Wh[c & 1][s][e][1]); };
  u32x4 R[4][4];
  bf16x8 A[8][4];
  TUnit U;
  u32x4 gs, s_wo = mk_srd(kp->w_out, FH * DM * 2);
  if (tj == 5) s_wo.z = 0;
  auto wo_first = [&](int k) { wo_piece(s_wo, lane, tj, rot4, k / 6, k % 6, w, (k < 6 ? L_W0 : L_W1 - 6 * 8192) + k * 8192); };
  const u32 hoff = lane < 32 ? WS_H + (u32)(((ti * 4 + (w >> 1)) * 4 * 6 + tj) * 1024 + (32 * (w & 1) + lane) * 16) : WS_END;
  const u32 st_v = WS_STATS + (u32)((ti * 6 + min(w, 5)) * 1024 + lane * 16);
  f32x4 acc2[2][2] = {}, acc1[2][2] = {}, ag = {};
  asm volatile("" : "+v"(acc2[0][0]), "+v"(acc2[0][1]), "+v"(acc2[1][0]), "+v"(acc2[1][1]));
  u32 tag;
  float bsp;
  asm volatile("s_waitcnt vmcnt(%4)\n\tv_readfirstlane_b32 %0, %2\n\tv_lshlrev_b32 %1, 16, %3\n\ts_nop 1"
               : "=s"(tag), "=v"(bsp) : "v"(ep_raw), "v"(bsp_raw), "i"(NPRE - 3) : "memory");
  tag += 1u;
  BARRIER();
#pragma unroll
  for (int k = 0; k < 12; ++k) f_read(0, k);
#pragma unroll
  for (int l = NPRE / 3; l < 4; ++l) { x_issue(l); w_issue(l); }
  sfor<0, 20>([&](auto IT) {
    constexpr int it = decltype(IT)::value, af = a_first(it), tu = (it - TU0) >> 1;
    constexpr int c0 = (it < 19 && it != 9) ? issued(it) - w_end(it + 1) : 63;
    constexpr int c1 = (it >= TU0 && it < TU0 + 8 && !(it & 1)) ? issued(it) - a_end(4 * tu + 3) : 63;
    constexpr int c2 = it == FLAG_IT ? issued(it) - issued(POLL_IT + 2) : 63;
    constexpr int cw = imin(c0, imin(c1, c2)), cw10 = issued(10) - w_end(10), cws = issued(POLL_IT + 1) - issued(POLL_IT - 2) - 1;
    static_assert(cw >= 0 && cw < 64 && cws < 64, "wait count");
    if (cw < 63) WAITV(cw);
    __builtin_amdgcn_s_waitcnt(0xC07F);
    HWBAR();
    if (it >= 10) STAMPS(1, it >= 10 ? it - 9 : 1); else STAMPS(4, it < 10 ? it + 1 : 1);
    SCHED();
    if (it == FLAG_IT) { gflag_st(s_ws, WS_FZ2 + (u32)((tj * 32 + ti) * 256), tag, smask(w == 0)); STAMP(5); }
    if (it == POLL_IT - 2) bload_sc1(gs, s_ws, st_v, 0);
    if (it == 18) {
      __builtin_amdgcn_raw_buffer_store_b128(u32x4{~0u, ~0u, ~0u, ~0u}, r_ws, hoff + 6144, 0, 16);
      __builtin_amdgcn_raw_buffer_store_b128(u32x4{~0u, ~0u, ~0u, ~0u}, r_ws, hoff + 2 * 6144, 0, 16);
      __builtin_amdgcn_raw_buffer_store_b128(u32x4{~0u, ~0u, ~0u, ~0u}, r_ws, hoff + 3 * 6144, 0, 16);
    }
    f32x2 sp[6];
    f32x4 rs[4];
    auto norm = [&](int k) {
      if (k == 0) {
        STAMP(4);
#pragma unroll
        for (int j = 0; j < 6; ++j) sp[j] = *lp<f32x2>(L_PART + lane * 8, 512 * j);
      } else if (k == 3) {
        const f32x2 tt = ((sp[0] + sp[1]) + (sp[2] + sp[3])) + (sp[4] + sp[5]);
        const float t1 = tt.x, t2 = tt.y;
        const float mean = t1 * (1.0f / FH);
        const float rsd = __builtin_amdgcn_rsqf(fmaxf(__builtin_fmaf(-mean, mean, t2 * (1.0f / FH)), 0.f) + 1e-5f);
        *lp<f32x2>(L_RS + lane * 8) = f32x2{rsd, -mean * rsd};
        CBAR();
        const u32 rb = opq(L_RS + (u32)((32 * mh + 8 * q) * 8));
#pragma unroll
        for (int j = 0; j < 4; ++j) rs[j] = *lp<f32x4>(rb, 16 * j);
      } else if (k == 8) {
        const u32 zo = WS_Z2 + (u32)(((tj * 8 + 2 * nq) * 64 + 4 * (ti >> 1) + q) * 1024 + (c16 + 16 * (2 * (ti & 1) + mh)) * 16);
#pragma unroll
        for (int e = 0; e < 2; ++e) {
          u32 o[4];
#pragma unroll
          for (int j = 0; j < 4; ++j)
            o[j] = pk2(__builtin_fmaf(acc2[j >> 1][e][2 * (j & 1)], rs[j][0], rs[j][1]), __builtin_fmaf(acc2[j >> 1][e][2 * (j & 1) + 1], rs[j][2], rs[j][3]));
          __builtin_amdgcn_raw_buffer_store_b128(u32x4{o[0], o[1], o[2], o[3]}, r_ws, zo + e * 65536, 0, 16);
        }
      }
      SCHED();
    };
    if (it == 9) {
      const u32 blk = (u32)(2 * ((nq + rot4) & 3));
      asm volatile("" : "+v"(acc1[0][0]), "+v"(acc1[0][1]), "+v"(acc1[1][0]), "+v"(acc1[1][1]), "+v"(ag));
      wb[0] = opq(wrow + (blk ^ wf) * 32);
      wb[1] = opq(wrow + ((blk + 1) ^ wf) * 32);
    }
    const u32 wso = line_of((it + 4) % 10) * (64 * 3072) + (it + 4 < 10 ? FH * 2 : 0);
    auto vmem = [&](int j) {
      const int jw = j - (it < 6 ? 1 : 0), ja = j - n_dma(it);
      if (it < 6 && j == 0) x_issue(it + 4);
      else if (j < n_dma(it) && jw == 0) { m0_set(L_RING + (it & 3) * CHUNK + w * 2048); asm volatile("s_nop 0"); dma16o<0>(s_w, w_v0, wso); }
      else if (j < n_dma(it)) dma16o<1024>(s_w, w_v1, wso);
      else if (ja < na(it)) a_load(R, s_wsp, a_v, a_sb, (af + ja) & 15);
      else if (ja - na(it) < nd(it)) {
        const int ja = j - n_dma(it) - na(it);
        const bool inx = it <= 17 && ja == 0;
        const int pc = inx ? it - 10 : 8 + 2 * (it - 16) + ja - (it <= 17 ? 1 : 0);
        dma16(s_wsp, (pc & 1) ? d_v1 : d_v0, (a_sb ^ 256u) + (u32)((32 * (pc >> 3) + 4 * ((pc >> 2) & 1)) * 4096 + ((pc >> 1) & 1) * 128),
              inx ? L_X + pc * 8192 + w * 1024 : L_RING + (it & 3) * CHUNK + w * 2048 + (pc & 1) * 1024);
      }
    };
    SCHED();
#pragma unroll
    for (int k = 0; k < 8; ++k) {
      const int s = k >> 2, b = (k >> 1) & 1, e = k & 1;
      if (it == POLL_IT + 1) norm(k);
      if (it < 10) mfma16_ip(acc2[b][e], Xf[it & 1][s][b], Wf(it, s, e));
      else mfma16_ip(acc1[b][e], Wf(it, s, e), Xf[it & 1][s][b]);
      SCHED();
      if (it < 19 && it != 9 && k < 6) { f_read(it + 1, 2 * k); f_read(it + 1, 2 * k + 1); }
      if (!(k & 1)) vmem(k >> 1);
      if (k == 7 && it != 9) vmem(4);
      if (it >= 10 && it < 19 && k == 5) g_read(it + 1);
      if (it >= TU0 && it < TU0 + 8 && k >= 6) {
        tstep(U, R[tu & 3], 2 * (it & 1) + k - 6);
        if ((it & 1) && k == 7) a_take(A, U, tu & 3);
      }
      SCHED();
    }
    if (it >= 10) mfma16_ip(ag, Gf[it & 1][0], Gf[it & 1][1]);
    if (it == POLL_IT + 1) norm(8);
    if (it == 9) {
      STAMP(2);
      float sv[16];
      asm volatile("s_nop 7" : "+v"(acc2[0][0]), "+v"(acc2[0][1]), "+v"(acc2[1][0]), "+v"(acc2[1][1]));
#pragma unroll
      for (int b = 0; b < 2; ++b) {
        a_load(R, s_wsp, a_v, a_sb, (af + 2 + 2 * b) & 15);
        const f32x4 g0 = gelu4(acc2[b][0]), g1 = gelu4(acc2[b][1]);
        acc2[b][0] = g0;
        acc2[b][1] = g1;
        const f32x4 s1 = g0 + g1, s2 = __builtin_elementwise_fma(g0, g0, g1 * g1);
#pragma unroll
        for (int r = 0; r < 4; ++r) { sv[8 * b + 2 * r] = s1[r]; sv[8 * b + 2 * r + 1] = s2[r]; }
        a_load(R, s_wsp, a_v, a_sb, (af + 3 + 2 * b) & 15);
        SCHED();
      }
      *lp<f32x4>(L_PART + (u32)(nq * 512 + (32 * mh + 8 * q) * 8 + a4 * 16)) = rowsum16(sv);
      SCHED();
      WAITV(cw10 - 4);
      BARRIER();
      if (w == 0) {
        const u32 pb = L_PART + lane * 8;
        const f32x2 t = (*lp<f32x2>(pb) + *lp<f32x2>(pb, 512)) + (*lp<f32x2>(pb, 1024) + *lp<f32x2>(pb, 1536));
        __builtin_amdgcn_raw_buffer_store_b128(u32x4{__float_as_uint(t.x), tag, __float_as_uint(t.y), tag}, r_ws, WS_STATS + (u32)((ti * 6 + tj) * 1024 + lane * 16), 0, 16);
      }
#pragma unroll
      for (int k = 0; k < 12; ++k) f_read(10, k);
      g_read(10);
#pragma unroll
      for (int k = 6; k < 10; ++k) a_load(R, s_wsp, a_v, a_sb, (af + k) & 15);
      STAMP(3);
    }
    if (it == POLL_IT) {
      WAITV_PIN(cws, gs);
      u32x4 g = gs;
#pragma nounroll
      for (u32 sp = 0; sp < 0x4000u && !__all(g.y == tag && g.w == tag); ++sp) {
        __builtin_amdgcn_s_sleep(2);
        g = __builtin_amdgcn_raw_buffer_load_b128(r_ws, st_v, 0, 16);
      }
      *lp<u32x2>(L_PART + (u32)(min(w, 5) * 512 + lane * 8)) = u32x2{g.x, g.z};
    }
  });
  asm volatile("s_nop 7" : "+v"(acc1[0][0]), "+v"(acc1[0][1]), "+v"(acc1[1][0]), "+v"(acc1[1][1]), "+v"(ag));
  STAMPS(1, 11);
  STAMPS(2, 9);
  STAMP(6);
  gflag_wait(s_ws, WS_FZ2 + (u32)((tj * 32 + 4 * ksl + (lane & 3)) * 256), tag);
  STAMP(7);
  {
    const u32 db = (u32)((q >> 1) * 16384 + (c16 & 7) * 128 + ((4 * (q & 1)) ^ (c16 >> 1)) * 16);
#pragma unroll
    for (int e = 0; e < 4; ++e) {
      const u32 ax = opq((db ^ (u32)(16 * e)) + L_X + w * 1024 + (c16 >> 3) * 8192), ar = opq((db ^ (u32)(16 * e)) + L_RING + w * 2048 + (c16 >> 3) * 1024);
      A[4 + e][0] = lds128(ax); A[4 + e][1] = lds128(ax, 32768); A[4 + e][2] = lds128(ar); A[4 + e][3] = lds128(ar, 32768);
    }
  }
  const u32 zb = WS_Z2 + (u32)((tj * 8 * 64 + 8 * ksl) * 1024 + lane * 16);
  u32x4 win[WIN];
  auto zso = [&](int n) { return (u32)((((n >> 4) + rot4) & 3) * 131072 + ((n >> 3) & 1) * 65536 + ((n & 4) ^ (4 * (tj & 1))) * 1024); };
  auto zload = [&](int n) { bload_sc1(win[n % WIN], s_ws, zb, zso(n) + (n & 3) * 1024); };
  *lp<float>(L_GP + (nq >> 1) * 256 + trow * 4 + (nq & 1) * 16) = ag[0];
#pragma unroll
  for (int k = 0; k < 4; ++k) {
    const int b = k >> 1, e = k & 1;
#pragma unroll
    for (int n = k * WIN / 4; n < (k + 1) * WIN / 4; ++n) zload(n);
    const f32x4 g = gelu4(acc1[b][e]);
    *lp<u32x2>(L_Z1 + (u32)((2 * mh * 4 + nq) * 1024 + lane * 8), b * 4096 + e * 512) = u32x2{pk2(g[0], g[1]), pk2(g[2], g[3])};
    SCHED();
  }
  const u32 rwb = opq(L_RED + (u32)(w * 4096 + lane * 16));
  const u32 rrb = opq(L_RED + (u32)((lane >> 5) * 16384 + (w >> 1) * 1024 + (32 * (w & 1) + (lane & 31)) * 16));
  f32x4 rv[4], se;
  u32x2 z1p[4];
  float og;
  auto z1_fetch = [&]() {
#pragma unroll
    for (int p = 0; p < 4; ++p) z1p[p] = *lp<u32x2>(L_Z1 + (u32)((w >> 1) * 4096 + (lane >> 5) * 512 + (32 * (w & 1) + (lane & 31)) * 8), p * 1024);
    const u32 gb = L_GP + (u32)((32 * (w >> 2) + 8 * (c16 >> 2) + 4 * ((w >> 1) & 1) + (c16 & 3)) * 4);
    rv[0][0] = *lp<float>(gb);
    rv[0][1] = *lp<float>(gb, 256);
  };
  auto gate_fin = [&]() {
    const float gate = __builtin_amdgcn_rcpf(1.0f + __builtin_amdgcn_exp2f((rv[0][0] + rv[0][1]) * -1.4426950409f));
    *lp<float>(L_GATE + (u32)(((w >> 1) * 16 + c16) * 4)) = gate;
    og = 1.0f - gate;
    bsp *= og;
    asm volatile("" : "+v"(og), "+v"(bsp));
  };
  auto red_ld = [&](int e) {
#pragma unroll
    for (int k = 0; k < 4; ++k) rv[k] = *lp<f32x4>(rrb, (e & 1) * 32768 + k * 4096);
  };
  auto red_add = [&](int e) {
    const f32x4 t = (rv[0] + rv[1]) + (rv[2] + rv[3]);
    if (!(e & 1)) { se = t; asm volatile("" : "+v"(se)); return; }
    float g[4];
#pragma unroll
    for (int r = 0; r < 4; ++r) {
      const auto x = __builtin_amdgcn_permlane32_swap(__float_as_uint(se[r]), __float_as_uint(t[r]), false, false);
      g[r] = __builtin_fmaf(__uint_as_float(x[0]) + __uint_as_float(x[1]), og, bsp);
    }
    const u32x2 z = z1p[e >> 1];
    const u32 o0 = min(pk2(bf2f(z.x & 0xffffu) * g[0], bfhi(z.x) * g[1]), 0xFFFFFFFEu),
              o1 = min(pk2(bf2f(z.y & 0xffffu) * g[2], bfhi(z.y) * g[3]), 0xFFFFFFFEu);
    const auto y0 = __builtin_amdgcn_permlane32_swap(o0, o0, false, false), y1 = __builtin_amdgcn_permlane32_swap(o1, o1, false, false);
    __builtin_amdgcn_raw_buffer_store_b128(u32x4{y0[0], y1[0], y0[1], y1[1]}, r_ws, hoff + (u32)((e >> 1) * 6144), 0, 16);
  };
  const u32 w0mask = smask(w == 0);
  f32x4 acc[2][4];
  u32x4 h0[6], s_h = s_ws;
  if (w >= 4 || tj == 5) s_h.z = 0;
  u32 pv;
  const u32 fh0 = WS_FH + (u32)((ti * 8 + min(lane & 7, 5)) * 256);
  sfor<0, 64>([&](auto IN) {
    constexpr int n = decltype(IN)::value, e = n >> 3, ks = n & 7;
    constexpr int zy = z_younger(n);
#define G2_STEP(C0, C1, C2, C3, TAIL) \
    "s_waitcnt vmcnt(%12)\n\tv_mfma_f32_16x16x32_bf16 %0, %4, %5, " C0 "\n\tv_mfma_f32_16x16x32_bf16 %1, %4, %6, " C1 "\n\t" \
    "v_mfma_f32_16x16x32_bf16 %2, %4, %7, " C2 "\n\tv_mfma_f32_16x16x32_bf16 %3, %4, %8, " C3 TAIL
#define G2_LOAD "\n\tbuffer_load_dwordx4 %4, %9, %10, %11 offen offset:%13 sc1"
#define G2_IN "v"(A[ks][0]), "v"(A[ks][1]), "v"(A[ks][2]), "v"(A[ks][3]), "v"(zb), "s"(s_ws), "s"(zso((n + WIN) & 63)), "i"(zy), "i"(((n + WIN) & 3) * 1024) : "memory"
    f32x4(&ac)[4] = acc[e & 1];
    if (ks && n + WIN < 64) asm volatile(G2_STEP("%0", "%1", "%2", "%3", G2_LOAD) : "+v"(ac[0]), "+v"(ac[1]), "+v"(ac[2]), "+v"(ac[3]), "+v"(win[n % WIN]) : G2_IN);
    else if (ks) asm volatile(G2_STEP("%0", "%1", "%2", "%3", "") : "+v"(ac[0]), "+v"(ac[1]), "+v"(ac[2]), "+v"(ac[3]), "+v"(win[n % WIN]) : G2_IN);
    else if (n + WIN < 64) asm volatile(G2_STEP("0", "0", "0", "0", G2_LOAD) : "=&v"(ac[0]), "=&v"(ac[1]), "=&v"(ac[2]), "=&v"(ac[3]), "+v"(win[n % WIN]) : G2_IN);
    else asm volatile(G2_STEP("0", "0", "0", "0", "") : "=&v"(ac[0]), "=&v"(ac[1]), "=&v"(ac[2]), "=&v"(ac[3]), "+v"(win[n % WIN]) : G2_IN);
    SCHED();
    if (e > 0 && ks < 4) *lp<f32x4>(rwb, ((e - 1) & 1) * 32768 + ks * 1024) = acc[(e - 1) & 1][ks & 3];
    if (ks == 5) {
      WAITL0();
      HWBAR();
      STAMPS(2, e + 1);
      if (e == 4) gflag_st(s_ws, WS_FH + (u32)((ti * 8 + tj) * 256), tag, w0mask);
      if (e > 0) red_ld(e - 1); else z1_fetch();
    }
    if (wo_at(n)) wo_first((n + 3) % 11);
    if (n == N_POLL) asm volatile("buffer_load_dword %0, %1, %2, 0 offen sc1" : "=v"(pv) : "v"(fh0), "s"(s_ws) : "memory");
    if (n == N_H0) { WAITV(63 - N_POLL - WIN + 1 + 7); gflag_check(pv, s_ws, fh0, tag); }
    if (n >= N_H0) bload_sc1(h0[(n + 60 - N_H0) % 6], s_h, (rwb & 1023u),
                                 WS_H + (u32)((ti * 4 + ((2 * (w & 1) + ((w >> 1) & 1)) ^ ((n - N_H0) & 1))) * 24576 + ((w >> 1) & 1) * 3072 + (n - N_H0) / 6 * 6144 + ((n - N_H0) % 6 >> 1) * 1024));
    if (ks == 7) { if (e > 0) red_add(e - 1); else gate_fin(); }
    SCHED();
  });
  asm volatile("s_nop 3" : "+v"(acc[1][0]), "+v"(acc[1][1]), "+v"(acc[1][2]), "+v"(acc[1][3]));
#pragma unroll
  for (int m = 0; m < 4; ++m) *lp<f32x4>(rwb, 32768 + m * 1024) = acc[1][m];
  WAITV(11);
  BARRIER();
  STAMP(8);
  STAMPS(2, 10);
  STAMPS(3, 8);
  red_ld(7);
  wo_piece(s_wo, lane, tj, rot4, 1, 5, w, L_W1B);
  red_add(7);
  HWBAR();
  asm volatile("s_waitcnt vmcnt(2)" : "+v"(h0[0]), "+v"(h0[1]), "+v"(h0[2]), "+v"(h0[3]), "+v"(h0[4]), "+v"(h0[5])::"memory");
  tail3(ws, tag, ti, tj, w, (int)opq(lane), h0 TSPASS);
}

__global__ __launch_bounds__(512, 2) void fused_kernel(Params P) {
  extern __shared__ __attribute__((aligned(16))) char smem[];
  const int lane = threadIdx.x & 63;
  const int w = __builtin_amdgcn_readfirstlane(threadIdx.x >> 6);
  const int b = blockIdx.x, xcd = b & 7, idx = b >> 3;
  STAMP(0);
  if (w == 4) *lp<u32>(L_FLG + 4 * lane) = 0;
  if (idx < 8) ssm_wg(xcd * 8 + idx, w, lane TSPASS);
  else gemm_wg(P, (xcd >> 1) * 8 + (idx - 8) / 3, (xcd & 1) * 3 + (idx - 8) % 3, w, lane TSPASS);
}

extern "C" void kernel_launch(void* const* d_in, const int* in_sizes, int n_in, void* d_out, int out_size, void* d_ws, size_t ws_size,
                              hipStream_t stream) {
  Params P;
  P.u = (const u16*)d_in[0]; P.Are = (const u16*)d_in[1]; P.Aim = (const u16*)d_in[2]; P.Bre = (const u16*)d_in[3];
  P.Bim = (const u16*)d_in[4]; P.Cre = (const u16*)d_in[5]; P.Cim = (const u16*)d_in[6]; P.logdt = (const u16*)d_in[7];
  P.dskip = (const u16*)d_in[8]; P.w_in = (const u16*)d_in[10]; P.w_sp = (const u16*)d_in[11]; P.b_sp = (const u16*)d_in[12];
  P.w_out = (const u16*)d_in[13]; P.gate_w = (const u16*)d_in[14]; P.out = (u16*)d_out; P.ws = (char*)d_ws;
  (void)hipFuncSetAttribute((const void*)fused_kernel, hipFuncAttributeMaxDynamicSharedMemorySize, L_TOTAL);
  fused_kernel<<<256, 512, L_TOTAL, stream>>>(P);
}
```
